```python
import torch

def _softmax(x, axis=-1):
    return torch.softmax(x, dim=axis)
def _gelu(x):
    return torch.nn.functional.gelu(x, approximate="tanh")
def _silu(x):
    return torch.nn.functional.silu(x)
def _sigmoid(x):
    return torch.sigmoid(x)
def _layernorm(x, axis=-1, eps=1e-5):
    m = x.mean(dim=axis, keepdim=True)
    v = x.var(dim=axis, keepdim=True, unbiased=False)
    return (x - m) / torch.sqrt(v + eps)
def _scatter_add(tgt, idx, src, axis=0):
    return tgt.clone().index_add_(axis, idx, src)
def _segment_sum(src, seg, n):
    out = torch.zeros((n,) + src.shape[1:], dtype=src.dtype)
    return out.index_add_(0, seg, src)
def _pad(x, pw):
    flat = tuple(v for pair in reversed(pw) for v in pair)
    return torch.nn.functional.pad(x, flat)

kernel_name = 'gated_mix__grouped_kv_mqa__dense_activation_relu__prompt'

def setup_inputs():

    _w = (lambda a: (lambda t: t.to(torch.bfloat16) if t.is_floating_point() else t)(torch.as_tensor(a)))
    def _A_setup():
        B, S, D, H = 1, 2048, 512, 32
        Dh = D // H
        return {
            "x": _w((lambda *s: torch.randn(*s, dtype=torch.bfloat16) * 0.05)(B, S, D)),
            "wq": _w((lambda *s: torch.randn(*s, dtype=torch.bfloat16) * 0.8)(D, H * Dh)),
            "wk": _w((lambda *s: torch.randn(*s, dtype=torch.bfloat16) * 0.8)(D, Dh)),
            "wv": _w((lambda *s: torch.randn(*s, dtype=torch.bfloat16) * 0.2)(D, Dh)),
            "wo": _w((lambda *s: torch.randn(*s, dtype=torch.bfloat16) * 0.2)(H * Dh, D)),
        }
    _A_in = _A_setup()

    def _B_setup():
        B, S, D, Dff = 1, 2048, 512, 1792
        return {
            "x": _w((lambda *s: torch.randn(*s, dtype=torch.bfloat16) * 0.05)(B, S, D)),
            "w1": _w((lambda *s: torch.randn(*s, dtype=torch.bfloat16) * 0.05)(D, Dff)),
            "w2": _w((lambda *s: torch.randn(*s, dtype=torch.bfloat16) * 0.05)(Dff, D)),
        }
    _B_in = _B_setup()

    return {
        "A_x": _A_in["x"],
        "A_wq": _A_in["wq"],
        "A_wk": _A_in["wk"],
        "A_wv": _A_in["wv"],
        "A_wo": _A_in["wo"],
        "B_x": _B_in["x"],
        "B_w1": _B_in["w1"],
        "B_w2": _B_in["w2"],
        "gate_w": _w((lambda *s: torch.randn(*s, dtype=torch.bfloat16) * 0.05)(512) * 16.0),
    }

def reference(A_x, A_wq, A_wk, A_wv, A_wo, B_x, B_w1, B_w2, gate_w):
    def _A_ref(x, wq, wk, wv, wo):
        B, S, D = x.shape
        H = 32
        Dh = D // H
        q = (lambda x, *d: x.permute(*d))((x @ wq).reshape(B, S, H, Dh), 0, 2, 1, 3)
        k = x @ wk
        v = x @ wv
        scores = torch.einsum("bhqd,bkd->bhqk", q, k) * (Dh ** -0.5)
        p = _softmax(scores, axis=-1)
        o = torch.einsum("bhqk,bkd->bhqd", p, v)
        return (lambda x, *d: x.permute(*d))(o, 0, 2, 1, 3).reshape(B, S, H * Dh) @ wo

    def _B_ref(x, w1, w2):
        h = torch.einsum("bsd,df->bsf", x, w1)
        zero = torch.zeros_like(h)
        h = torch.maximum(h, zero)
        return torch.einsum("bsf,fd->bsd", h, w2)

    x = A_x
    g = _sigmoid((x * gate_w).sum(-1, keepdims=True))
    a = _A_ref(x, A_wq, A_wk, A_wv, A_wo)
    b = _B_ref(x, B_w1, B_w2)
    return g * a + (1 - g) * b
```

```cpp
#include <hip/hip_runtime.h>
#include <hip/hip_bf16.h>
#include <stdint.h>

typedef __attribute__((ext_vector_type(8))) __bf16 bf16x8;
typedef __attribute__((ext_vector_type(16))) __bf16 bf16x16;
typedef __attribute__((ext_vector_type(4))) short s16x4;
typedef __attribute__((ext_vector_type(16))) float f32x16;
typedef __attribute__((ext_vector_type(8))) float f32x8;
typedef __attribute__((ext_vector_type(4))) float f32x4;
typedef __attribute__((ext_vector_type(4))) unsigned u32x4;
typedef __attribute__((ext_vector_type(2))) unsigned u32x2;
#define LAS __attribute__((address_space(3)))
#define GAS __attribute__((address_space(1)))
#define DI __device__ __forceinline__
typedef unsigned short u16;

constexpr int S = 2048, D = 512, H = 32, DH = 16, DFF = 1792;
constexpr int KSTEPS_A = D / 16;
constexpr int KSTEPS_H = DFF / 16;
constexpr int KSTEPS_B = KSTEPS_H + D / 16;
constexpr size_t WS_QF = 0;
constexpr size_t WS_KF = WS_QF + 64 * 32 * 1024;
constexpr size_t WS_VF = WS_KF + 64 * 1024;
constexpr size_t WS_AF = WS_VF + 64 * 1024;
constexpr size_t WS_WF = WS_AF + (size_t)64 * KSTEPS_B * 1024;
constexpr size_t WS_G = WS_WF + (size_t)16 * KSTEPS_B * 1024;
constexpr size_t WS_KN = WS_G + 2048 * 4;

constexpr float QSCALE = 0.25f * 1.4426950408889634f;

DI f32x16 mfma32(u32x4 a, u32x4 b, f32x16 c) {
  return __builtin_amdgcn_mfma_f32_32x32x16_bf16(__builtin_bit_cast(bf16x8, a), __builtin_bit_cast(bf16x8, b), c, 0, 0, 0);
}
DI f32x16 smfmac_hi(u32x4 a, f32x8 b, f32x16 c, int idx) {
  return __builtin_amdgcn_smfmac_f32_32x32x32_bf16(__builtin_bit_cast(bf16x8, a), __builtin_bit_cast(bf16x16, b), c, idx, 0, 0);
}
typedef __attribute__((ext_vector_type(2))) __bf16 bf16x2;
typedef __attribute__((ext_vector_type(2))) float f32x2;
DI unsigned pk2(float lo, float hi) {
  f32x2 v = {lo, hi};
  return __builtin_bit_cast(unsigned, __builtin_convertvector(v, bf16x2));
}
DI u32x4 pack8(const f32x16& x, int o, float sc) {
  u32x4 p;
  p[0] = pk2(x[o + 0] * sc, x[o + 1] * sc); p[1] = pk2(x[o + 2] * sc, x[o + 3] * sc);
  p[2] = pk2(x[o + 4] * sc, x[o + 5] * sc); p[3] = pk2(x[o + 6] * sc, x[o + 7] * sc);
  return p;
}
DI float bflo(unsigned u) { return __uint_as_float(u << 16); }
DI float bfhi(unsigned u) { return __uint_as_float(u & 0xffff0000u); }

DI void glds16(const void* base, unsigned voff, unsigned lds_base) {
  unsigned keep;
  asm volatile("s_mov_b32 %0, m0\n\ts_mov_b32 m0, %3\n\ts_nop 0\n\tglobal_load_lds_dwordx4 %1, %2\n\ts_mov_b32 m0, %0"
               : "=&s"(keep) : "v"(voff), "s"(base), "s"(lds_base) : "memory");
}
#define WAIT_VM(n) asm volatile("s_waitcnt vmcnt(" #n ")" ::: "memory")
#define WAIT_LGKM0() asm volatile("s_waitcnt lgkmcnt(0)" ::: "memory")
#define BARRIER() asm volatile("s_waitcnt lgkmcnt(0)\n\ts_barrier" ::: "memory")
template <int N> DI void wait_vm() { asm volatile("s_waitcnt vmcnt(%0)" ::"n"(N) : "memory"); }

typedef short v4i16_t __attribute__((ext_vector_type(4)));
DI u32x2 tr_read(const LAS char* p) { return __builtin_bit_cast(u32x2, __builtin_amdgcn_ds_read_tr16_b64_v4i16((LAS v4i16_t*)p)); }
DI u32x4 lds128(const LAS char* p) { return *(const LAS u32x4*)p; }

constexpr int RING = 8;
constexpr int L1_X = 0, L1_RING = 65536, L1_GW = L1_RING + 12 * RING * 1024 - 2 * RING * 1024 + 2 * RING * 1024;
constexpr int L1_RING2 = L1_RING + 10 * RING * 1024;
constexpr int L1_MISC = L1_RING2 + 2 * 4096;
constexpr int L1_BYTES = L1_MISC + 2048;

__global__ __launch_bounds__(768) void k1_proj(const u16* __restrict__ x, const u16* __restrict__ wq, const u16* __restrict__ wk,
                                               const u16* __restrict__ wv, const u16* __restrict__ wo, const u16* __restrict__ w1,
                                               const u16* __restrict__ w2, const u16* __restrict__ gw, char* __restrict__ ws) {
  extern __shared__ __attribute__((aligned(16))) char smem[];
  const LAS char* lds = (const LAS char*)smem;
  const unsigned lds0 = (unsigned)(uintptr_t)smem;
  const int b = blockIdx.x, xcd = b & 7, t = b >> 3, bi = xcd + 8 * (t >> 3), bj = t & 7;
  const int lane = threadIdx.x & 63, w = __builtin_amdgcn_readfirstlane(threadIdx.x >> 6);
  const int r = lane & 31, h = lane >> 5;
  if (w < 8) {
    const int row = w * 8 + (lane >> 3);
    const unsigned voff = (unsigned)(row * D * 2 + (((lane & 7) ^ ((row >> 1) & 7)) * 16));
    const char* xb = (const char*)(x + (size_t)bi * 64 * D);
#pragma unroll
    for (int kc = 0; kc < 8; ++kc) glds16(xb + kc * 128, voff, lds0 + L1_X + (w * 8 + kc) * 1024);
  } else if (w == 10) {
    glds16(gw, lane * 16, lds0 + L1_MISC);
  }
  const int trq = (lane & 15) >> 2, trp = lane & 3, trcb = (lane >> 4) & 1;
  if (w < 10) {
    const char* wbase; int pitch;
    if (w < 2) { wbase = (const char*)(wq + bj * 64 + w * 32); pitch = D * 2; }
    else { const int ct = w < 8 ? w - 2 : 6; wbase = (const char*)(w1 + bj * 224 + ct * 32); pitch = DFF * 2; }
    const unsigned wvoff = (unsigned)((lane >> 2) * pitch + (lane & 3) * 16);
    const unsigned ring = lds0 + L1_RING + w * RING * 1024;
#pragma unroll
    for (int s = 0; s < RING; ++s) glds16(wbase + (size_t)s * 16 * pitch, wvoff, ring + s * 1024);
    wait_vm<RING>();
    BARRIER();
    const LAS char* wrd = lds + L1_RING + w * RING * 1024 + (8 * h + trq) * 64 + trcb * 32 + trp * 8;
    const int rt0 = w < 8 ? 0 : w - 8;
    const int R0 = rt0 * 32 + r;
    const LAS char* xrd = lds + L1_X + (R0 >> 3) * 8192 + (R0 & 7) * 128;
    const int swz = (r >> 1) & 7;
    f32x16 acc0 = {}, acc1 = {};
#pragma unroll
    for (int s = 0; s < KSTEPS_A; ++s) {
      if (s <= KSTEPS_A - RING) wait_vm<RING - 1>();
      else {
        switch (KSTEPS_A - 1 - s) { case 6: wait_vm<6>(); break; case 5: wait_vm<5>(); break; case 4: wait_vm<4>(); break; case 3: wait_vm<3>(); break;
          case 2: wait_vm<2>(); break; case 1: wait_vm<1>(); break; default: wait_vm<0>(); break; }
      }
      const u32x2 wl = tr_read(wrd + (s % RING) * 1024), wh = tr_read(wrd + (s % RING) * 1024 + 256);
      const int xo = (s >> 2) * 1024 + (((2 * (s & 3) + h) ^ swz) * 16);
      const u32x4 x0 = lds128(xrd + xo);
      u32x4 x1;
      if (w < 8) x1 = lds128(xrd + xo + 32768);
      const u32x4 wf = {wl[0], wl[1], wh[0], wh[1]};
      if (s + RING < KSTEPS_A) { WAIT_LGKM0(); glds16(wbase + (size_t)(s + RING) * 16 * pitch, wvoff, ring + (s % RING) * 1024); }
      acc0 = mfma32(wf, x0, acc0);
      if (w < 8) acc1 = mfma32(wf, x1, acc1);
    }
    BARRIER();
    const float* gl = (const float*)(smem + L1_MISC + 1024);
    if (w < 2) {
      char* q0 = ws + WS_QF + ((size_t)(bi * 2) * 32 + bj * 4 + w * 2) * 1024 + lane * 16;
      *(u32x4*)(q0) = pack8(acc0, 0, QSCALE); *(u32x4*)(q0 + 1024) = pack8(acc0, 8, QSCALE);
      *(u32x4*)(q0 + 32768) = pack8(acc1, 0, QSCALE); *(u32x4*)(q0 + 32768 + 1024) = pack8(acc1, 8, QSCALE);
    } else {
      const int ct = w < 8 ? w - 2 : 6;
      char* h0 = ws + WS_AF + ((size_t)(bi * 2 + rt0) * KSTEPS_B + (bj * 7 + ct) * 2) * 1024 + lane * 16;
#pragma unroll
      for (int i = 0; i < 16; ++i) { acc0[i] = fmaxf(acc0[i], 0.f); acc1[i] = fmaxf(acc1[i], 0.f); }
      const float s0 = 1.f - gl[rt0 * 32 + r];
      *(u32x4*)(h0) = pack8(acc0, 0, s0); *(u32x4*)(h0 + 1024) = pack8(acc0, 8, s0);
      if (w < 8) {
        const float s1 = 1.f - gl[32 + r];
        *(u32x4*)(h0 + KSTEPS_B * 1024) = pack8(acc1, 0, s1); *(u32x4*)(h0 + KSTEPS_B * 1024 + 1024) = pack8(acc1, 8, s1);
      }
    }
  } else {
    const int rt = w - 10;
    const bool kv = (bj < 4) && ((bj & 1) == rt);
    const int orient = bj >> 1;
    const unsigned ring = lds0 + L1_RING2 + rt * 4096;
    const char* kvbase = (const char*)wk;
    const unsigned kvoff = (unsigned)((lane >> 2) * 32 + (lane & 1) * 16) + (((lane & 2) != 0) ? (unsigned)((const char*)wv - (const char*)wk) : 0u);
    (void)kvbase; (void)kvoff;
    const char* kvp = (const char*)((lane & 2) ? wv : wk) + (lane >> 2) * 32 + (lane & 1) * 16;
    auto kvdma = [&](int s) {
      unsigned keep; const char* p = kvp + s * 512; const unsigned dst = ring + (s & 3) * 1024;
      asm volatile("s_mov_b32 %0, m0\n\ts_mov_b32 m0, %2\n\ts_nop 0\n\tglobal_load_lds_dwordx4 %1, off\n\ts_mov_b32 m0, %0" : "=&s"(keep) : "v"(p), "s"(dst) : "memory");
    };
    if (kv) { kvdma(0); kvdma(1); kvdma(2); kvdma(3); wait_vm<4>(); } else wait_vm<0>();
    BARRIER();
    const int R0 = rt * 32 + r;
    const LAS char* xrd = lds + L1_X + (R0 >> 3) * 8192 + (R0 & 7) * 128;
    const int swz = (r >> 1) & 7;
    const LAS char* wrd = lds + L1_RING2 + rt * 4096 + (8 * h + trq) * 64 + trcb * 32 + trp * 8;
    f32x16 accg = {}, acck = {};
#pragma unroll 4
    for (int s = 0; s < KSTEPS_A; ++s) {
      const int xo = (s >> 2) * 1024 + (((2 * (s & 3) + h) ^ swz) * 16);
      const u32x4 x0 = lds128(xrd + xo);
      const u32x4 gf = lds128(lds + L1_MISC + s * 32 + h * 16);
      accg = mfma32(gf, x0, accg);
      if (kv) {
        if (s <= KSTEPS_A - 4) wait_vm<3>(); else wait_vm<0>();
        const u32x2 wl = tr_read(wrd + (s & 3) * 1024), wh = tr_read(wrd + (s & 3) * 1024 + 256);
        const u32x4 wf = {wl[0], wl[1], wh[0], wh[1]};
        if (s + 4 < KSTEPS_A) { WAIT_LGKM0(); kvdma(s + 4); }
        if (orient == 0) acck = mfma32(wf, x0, acck); else acck = mfma32(x0, wf, acck);
      }
    }
    const float g = 1.f / (1.f + __expf(-accg[0]));
    if (h == 0) { ((float*)(smem + L1_MISC + 1024))[rt * 32 + r] = g; if (bj == 0) ((float*)(ws + WS_G))[bi * 64 + rt * 32 + r] = g; }
    BARRIER();
    if (kv) {
      const int kt = bi * 2 + rt;
      if (orient == 0) {
        const u32x4 kf = pack8(acck, 0, 1.f);
        const int slot = (r & 19) | ((r & 4) << 1) | ((r & 8) >> 1);
        *(u32x4*)(ws + WS_KF + kt * 1024 + (h * 32 + slot) * 16) = kf;
        float n2 = 0.f;
#pragma unroll
        for (int i = 0; i < 4; ++i) { const float a = bflo(kf[i]), c = bfhi(kf[i]); n2 += a * a + c * c; }
        n2 += __shfl_xor(n2, 32);
#pragma unroll
        for (int o = 16; o; o >>= 1) n2 = fmaxf(n2, __shfl_xor(n2, o));
        if (lane == 0) ((float*)(ws + WS_KN))[kt] = n2;
      } else if (r >= 16) {
        char* vp = ws + WS_VF + kt * 1024 + h * 256 + (r - 16) * 16;
        *(u32x4*)(vp) = pack8(acck, 0, 1.f); *(u32x4*)(vp + 512) = pack8(acck, 8, 1.f);
      }
    }
    const LAS char* prd = lds + L1_RING2 + rt * 4096 + (4 * h + trq) * 64 + trcb * 32 + trp * 8;
    for (int k = rt; k < 9; k += 2) {
      const int blk = b * 9 + k, ct = blk / KSTEPS_B, ks = blk % KSTEPS_B;
      const char* src = ks < KSTEPS_H ? (const char*)(w2 + (size_t)ks * 16 * D + ct * 32) : (const char*)(wo + (size_t)(ks - KSTEPS_H) * 16 * D + ct * 32);
      glds16(src, (unsigned)((lane >> 2) * D * 2 + (lane & 3) * 16), ring);
      wait_vm<0>();
      const u32x2 wl = tr_read(prd), wh = tr_read(prd + 512);
      const u32x4 wf = {wl[0], wl[1], wh[0], wh[1]};
      *(u32x4*)(ws + WS_WF + (size_t)blk * 1024 + lane * 16) = wf;
      WAIT_LGKM0();
    }
  }
}

constexpr int L2_K = 0, L2_V = 65536, L2_ONES = 131072, L2_BYTES = L2_ONES + 2048;
#define SBAR() __builtin_amdgcn_sched_barrier(0)
DI f32x8 lo8(const f32x16& v) { return __builtin_shufflevector(v, v, 0, 1, 2, 3, 4, 5, 6, 7); }
DI f32x8 hi8(const f32x16& v) { return __builtin_shufflevector(v, v, 8, 9, 10, 11, 12, 13, 14, 15); }

__global__ __launch_bounds__(512) void k2_attn(char* __restrict__ ws) {
  extern __shared__ __attribute__((aligned(16))) char smem[];
  const LAS char* lds = (const LAS char*)smem;
  const unsigned lds0 = (unsigned)(uintptr_t)smem;
  const int b = blockIdx.x, xcd = b & 7, t = b >> 3, bi = xcd + 8 * (t >> 3), bj = t & 7;
  const int lane = threadIdx.x & 63, w = __builtin_amdgcn_readfirstlane(threadIdx.x >> 6);
  const int r = lane & 31, h = lane >> 5;
#pragma unroll
  for (int i = 0; i < 8; ++i) {
    glds16(ws + WS_KF + (i * 8 + w) * 1024, lane * 16, lds0 + L2_K + (i * 8 + w) * 1024);
    glds16(ws + WS_VF + (i * 8 + w) * 1024, lane * 16, lds0 + L2_V + (i * 8 + w) * 1024);
  }
  ((unsigned*)(smem + L2_ONES))[threadIdx.x] = 0x3f803f80u;
  const int rt = bi * 2 + (w & 1), head = bj * 4 + (w >> 1);
  const u32x4 qf = *(const u32x4*)(ws + WS_QF + ((size_t)rt * 32 + head) * 1024 + lane * 16);
  float kn = ((const float*)(ws + WS_KN))[lane];
  const float g = ((const float*)(ws + WS_G))[rt * 32 + r];
#pragma unroll
  for (int o = 32; o; o >>= 1) kn = fmaxf(kn, __shfl_xor(kn, o));
  float qn = 0.f;
#pragma unroll
  for (int i = 0; i < 4; ++i) { const float a = bflo(qf[i]), c = bfhi(qf[i]); qn += a * a + c * c; }
  qn += __shfl_xor(qn, 32);
  float m = sqrtf(qn * kn);
  wait_vm<0>();
  BARRIER();
  const int idx = 0xdddd;
  f32x16 o;
  for (int attempt = 0;; ++attempt) {
    f32x16 nm;
#pragma unroll
    for (int i = 0; i < 16; ++i) nm[i] = -m;
    o = f32x16{};
    const LAS char* kp = lds + L2_K + lane * 16;
    const LAS char* vp = r < 16 ? lds + L2_V + h * 256 + r * 16 : lds + L2_ONES + (r - 16) * 16;
    const int vinc = r < 16 ? 2048 : 0;
    f32x16 sa = mfma32(lds128(kp), qf, nm), sb;
    for (int kt = 0; kt < 64; kt += 2) {
      sb = mfma32(lds128(kp + 1024), qf, nm);
      const u32x4 v0 = lds128(vp), v1 = lds128(vp + 512), v2 = lds128(vp + 1024), v3 = lds128(vp + 1536);
      const u32x4 kn2 = lds128(kp + (kt + 2 < 64 ? 2048 : 0));
#pragma unroll
      for (int i = 0; i < 16; ++i) sa[i] = __builtin_amdgcn_exp2f(sa[i]);
      o = smfmac_hi(v0, lo8(sa), o, idx);
      o = smfmac_hi(v1, hi8(sa), o, idx);
      sa = mfma32(kn2, qf, nm);
#pragma unroll
      for (int i = 0; i < 16; ++i) sb[i] = __builtin_amdgcn_exp2f(sb[i]);
      o = smfmac_hi(v2, lo8(sb), o, idx);
      o = smfmac_hi(v3, hi8(sb), o, idx);
      kp += 2048; vp += vinc;
    }
    const bool bad = !(o[8] >= 0x1p-64f);
    if (attempt == 1 || !__any(bad)) break;
    float mx = -INFINITY;
    const f32x16 z = {};
    for (int kt = 0; kt < 64; ++kt) {
      const f32x16 s = mfma32(lds128(lds + L2_K + lane * 16 + kt * 1024), qf, z);
#pragma unroll
      for (int i = 0; i < 16; ++i) mx = fmaxf(mx, s[i]);
    }
    m = fmaxf(mx, __shfl_xor(mx, 32));
  }
  const float sc = g / o[8];
  *(u32x4*)(ws + WS_AF + ((size_t)rt * KSTEPS_B + KSTEPS_H + head) * 1024 + lane * 16) = pack8(o, 0, sc);
}

constexpr int RP = 68;
constexpr int L3_BYTES = 8 * 64 * RP * 4;
__global__ __launch_bounds__(512) void k3_out(const char* __restrict__ ws, u16* __restrict__ out) {
  extern __shared__ __attribute__((aligned(16))) char smem[];
  const int b = blockIdx.x, xcd = b & 7, t = b >> 3, bi = xcd + 8 * (t >> 3), bj = t & 7;
  const int lane = threadIdx.x & 63, w = __builtin_amdgcn_readfirstlane(threadIdx.x >> 6);
  const int r = lane & 31, h = lane >> 5;
  constexpr int NS = KSTEPS_B / 8;
  constexpr int WIN = 6;
  const char* ap = ws + WS_AF + ((size_t)(bi * 2) * KSTEPS_B + w * NS) * 1024 + lane * 16;
  const char* wp = ws + WS_WF + ((size_t)(bj * 2) * KSTEPS_B + w * NS) * 1024 + lane * 16;
  u32x4 fa0[WIN], fa1[WIN], fw0[WIN], fw1[WIN];
#pragma unroll
  for (int s = 0; s < WIN; ++s) {
    fa0[s] = *(const u32x4*)(ap + s * 1024); fa1[s] = *(const u32x4*)(ap + s * 1024 + KSTEPS_B * 1024);
    fw0[s] = *(const u32x4*)(wp + s * 1024); fw1[s] = *(const u32x4*)(wp + s * 1024 + KSTEPS_B * 1024);
  }
  f32x16 c00 = {}, c01 = {}, c10 = {}, c11 = {};
#pragma unroll
  for (int s = 0; s < NS; ++s) {
    const int k = s % WIN;
    const u32x4 a0 = fa0[k], a1 = fa1[k], w0 = fw0[k], w1 = fw1[k];
    c00 = mfma32(w0, a0, c00); c01 = mfma32(w0, a1, c01); c10 = mfma32(w1, a0, c10); c11 = mfma32(w1, a1, c11);
    if (s + WIN < NS) {
      fa0[k] = *(const u32x4*)(ap + (s + WIN) * 1024); fa1[k] = *(const u32x4*)(ap + (s + WIN) * 1024 + KSTEPS_B * 1024);
      fw0[k] = *(const u32x4*)(wp + (s + WIN) * 1024); fw1[k] = *(const u32x4*)(wp + (s + WIN) * 1024 + KSTEPS_B * 1024);
    }
    SBAR();
  }
  float* red = (float*)smem + w * 64 * RP;
#pragma unroll
  for (int gq = 0; gq < 4; ++gq) {
    const int n = 8 * gq + 4 * h;
    *(f32x4*)(red + r * RP + n) = f32x4{c00[4 * gq], c00[4 * gq + 1], c00[4 * gq + 2], c00[4 * gq + 3]};
    *(f32x4*)(red + (32 + r) * RP + n) = f32x4{c01[4 * gq], c01[4 * gq + 1], c01[4 * gq + 2], c01[4 * gq + 3]};
    *(f32x4*)(red + r * RP + 32 + n) = f32x4{c10[4 * gq], c10[4 * gq + 1], c10[4 * gq + 2], c10[4 * gq + 3]};
    *(f32x4*)(red + (32 + r) * RP + 32 + n) = f32x4{c11[4 * gq], c11[4 * gq + 1], c11[4 * gq + 2], c11[4 * gq + 3]};
  }
  __syncthreads();
  const int pos = threadIdx.x >> 3, n0 = (threadIdx.x & 7) * 8;
  f32x4 s0 = {}, s1 = {};
#pragma unroll
  for (int k = 0; k < 8; ++k) {
    const float* p = (const float*)smem + k * 64 * RP + pos * RP + n0;
    s0 += *(const f32x4*)p; s1 += *(const f32x4*)(p + 4);
  }
  u32x4 res = {pk2(s0[0], s0[1]), pk2(s0[2], s0[3]), pk2(s1[0], s1[1]), pk2(s1[2], s1[3])};
  *(u32x4*)(out + (size_t)(bi * 64 + pos) * D + bj * 64 + n0) = res;
}

extern "C" void kernel_launch(void* const* d_in, const int* in_sizes, int n_in, void* d_out, int out_size, void* d_ws, size_t ws_size,
                              hipStream_t stream) {
  const u16* x = (const u16*)d_in[0]; const u16* wq = (const u16*)d_in[1]; const u16* wk = (const u16*)d_in[2];
  const u16* wv = (const u16*)d_in[3]; const u16* wo = (const u16*)d_in[4]; const u16* w1 = (const u16*)d_in[6];
  const u16* w2 = (const u16*)d_in[7]; const u16* gw = (const u16*)d_in[8];
  (void)hipFuncSetAttribute((const void*)k1_proj, hipFuncAttributeMaxDynamicSharedMemorySize, L1_BYTES);
  (void)hipFuncSetAttribute((const void*)k2_attn, hipFuncAttributeMaxDynamicSharedMemorySize, L2_BYTES);
  (void)hipFuncSetAttribute((const void*)k3_out, hipFuncAttributeMaxDynamicSharedMemorySize, L3_BYTES);
  k1_proj<<<256, 768, L1_BYTES, stream>>>(x, wq, wk, wv, wo, w1, w2, gw, (char*)d_ws);
  k2_attn<<<256, 512, L2_BYTES, stream>>>((char*)d_ws);
  k3_out<<<256, 512, L3_BYTES, stream>>>((const char*)d_ws, (u16*)d_out);
}
```

```cpp
#include <hip/hip_runtime.h>
#include <hip/hip_bf16.h>
#include <stdint.h>

typedef __attribute__((ext_vector_type(8))) __bf16 bf16x8;
typedef __attribute__((ext_vector_type(16))) __bf16 bf16x16;
typedef __attribute__((ext_vector_type(2))) __bf16 bf16x2;
typedef __attribute__((ext_vector_type(32))) float f32x32;
typedef __attribute__((ext_vector_type(16))) float f32x16;
typedef __attribute__((ext_vector_type(8))) float f32x8;
typedef __attribute__((ext_vector_type(4))) float f32x4;
typedef __attribute__((ext_vector_type(2))) float f32x2;
typedef __attribute__((ext_vector_type(32))) unsigned u32x32;
typedef __attribute__((ext_vector_type(16))) unsigned u32x16;
typedef __attribute__((ext_vector_type(8))) unsigned u32x8;
typedef __attribute__((ext_vector_type(4))) unsigned u32x4;
typedef __attribute__((ext_vector_type(2))) unsigned u32x2;
typedef __attribute__((ext_vector_type(2))) short s16x2;
typedef short v4i16_t __attribute__((ext_vector_type(4)));
#define LAS __attribute__((address_space(3)))
#define DI __device__ __forceinline__
typedef unsigned short u16;

constexpr int D = 512, DFF = 1792;
constexpr int KS_H = DFF / 16;
constexpr int KS_B = KS_H + D / 16;
constexpr int WS_EP = 0;
constexpr int WS_FKV = 1024;
constexpr int WS_KV = 32768;
constexpr int WS_FA = WS_KV + 128 * 1024;
constexpr int WS_AF = WS_FA + 256 * 128;
constexpr int WS_BYTES = WS_AF + 64 * KS_B * 1024;
constexpr unsigned EMPTY = 0xFFFFFFFFu;
constexpr int L_X = 0, L_WQ = 65536, L_WKV = 131072, L_QF = 147456, L_RED = 155648, L_GW = 159744, L_G = 160768, L_CNT = 161024, L_BYTES = 161280;

constexpr float QSCALE = 0.25f * 1.4426950408889634f;
constexpr unsigned SPIN_CAP = 1u << 18;

DI f32x16 mfma32(u32x4 a, u32x4 b, f32x16 c) {
  return __builtin_amdgcn_mfma_f32_32x32x16_bf16(__builtin_bit_cast(bf16x8, a), __builtin_bit_cast(bf16x8, b), c, 0, 0, 0);
}
DI f32x4 mfma16(u32x4 a, u32x4 b, f32x4 c) {
  return __builtin_amdgcn_mfma_f32_16x16x32_bf16(__builtin_bit_cast(bf16x8, a), __builtin_bit_cast(bf16x8, b), c, 0, 0, 0);
}
DI f32x16 smfmac_hi(u32x4 a, f32x8 b, f32x16 c, int idx) {
  return __builtin_amdgcn_smfmac_f32_32x32x32_bf16(__builtin_bit_cast(bf16x8, a), __builtin_bit_cast(bf16x16, b), c, idx, 0, 0);
}
DI unsigned pk2(float lo, float hi) { f32x2 v = {lo, hi}; return __builtin_bit_cast(unsigned, __builtin_convertvector(v, bf16x2)); }
DI u32x4 pack8(const f32x16& x, int o, float sc) {
  u32x4 p;
  p[0] = pk2(x[o + 0] * sc, x[o + 1] * sc); p[1] = pk2(x[o + 2] * sc, x[o + 3] * sc);
  p[2] = pk2(x[o + 4] * sc, x[o + 5] * sc); p[3] = pk2(x[o + 6] * sc, x[o + 7] * sc);
  return p;
}
DI u32x4 pack8relu(const f32x16& x, int o) {
  u32x4 p;
#pragma unroll
  for (int i = 0; i < 4; ++i) {
    const s16x2 v = __builtin_bit_cast(s16x2, pk2(x[o + 2 * i], x[o + 2 * i + 1]));
    p[i] = __builtin_bit_cast(unsigned, __builtin_elementwise_max(v, s16x2{0, 0}));
  }
  return p;
}
DI f32x8 lo8(const f32x16& v) { return __builtin_shufflevector(v, v, 0, 1, 2, 3, 4, 5, 6, 7); }
DI f32x8 hi8(const f32x16& v) { return __builtin_shufflevector(v, v, 8, 9, 10, 11, 12, 13, 14, 15); }

DI void glds16(const void* base, unsigned voff, unsigned lds_base) {
  asm volatile("s_mov_b32 m0, %2\n\ts_nop 0\n\tglobal_load_lds_dwordx4 %0, %1" :: "v"(voff), "s"(base), "s"(lds_base) : "memory");
}
template <int N> DI void wait_vm() { asm volatile("s_waitcnt vmcnt(%0)" ::"n"(N) : "memory"); }
#define WAIT_LGKM0() asm volatile("s_waitcnt lgkmcnt(0)" ::: "memory")
#define BARRIER() asm volatile("s_waitcnt lgkmcnt(0)\n\ts_barrier" ::: "memory")
#define SBAR() __builtin_amdgcn_sched_barrier(0)
DI u32x2 tr_read(const LAS char* p) { return __builtin_bit_cast(u32x2, __builtin_amdgcn_ds_read_tr16_b64_v4i16((LAS v4i16_t*)p)); }
DI u32x4 tr_frag(const LAS char* p, int second) { const u32x2 a = tr_read(p), b = tr_read(p + second); return u32x4{a[0], a[1], b[0], b[1]}; }
DI u32x4 lds128(const LAS char* p) { return *(const LAS u32x4*)p; }
typedef __amdgpu_buffer_rsrc_t rsrc_t;
constexpr int SC1 = 16;
DI void ld_sc1(u32x4& dst, u32x4 rs, unsigned voff, unsigned soff) {
  asm volatile("buffer_load_dwordx4 %0, %1, %2, %3 offen sc1" : "=v"(dst) : "v"(voff), "s"(rs), "s"(soff) : "memory");
}
template <int SLEEP> DI void lds_wait(LAS unsigned* cnt, unsigned want) {
#pragma unroll 1
  for (unsigned spins = 0; spins < SPIN_CAP * 16; ++spins) {
    if (__builtin_amdgcn_readfirstlane(__hip_atomic_load(cnt, __ATOMIC_RELAXED, __HIP_MEMORY_SCOPE_WORKGROUP)) >= want) break;
    asm volatile("" ::: "memory");
    if (SLEEP) __builtin_amdgcn_s_sleep(SLEEP);
  }
  asm volatile("" ::: "memory");
}
constexpr int HOLD_J = 18, HOLD_N = 2;
constexpr int FPOLL = 20, FCHECK = 33;
constexpr int THR = 6;
constexpr int CODE_TOUCH = 24576;
#define TL(i) "s_load_dword %0, %1, " #i "*128\n\t"
#define TOUCH14(d, p) asm volatile(TL(0) TL(1) TL(2) TL(3) TL(4) TL(5) TL(6) TL(7) TL(8) TL(9) TL(10) TL(11) TL(12) TL(13) : "=&s"(d) : "s"(p) : "memory")
DI void wait_two_flags(rsrc_t rs, int off, unsigned tag, int lane) {
#pragma unroll 1
  for (unsigned spins = 0; spins < SPIN_CAP; ++spins) {
    const unsigned f = __builtin_amdgcn_raw_buffer_load_b32(rs, off + (lane & 1) * 128, 0, SC1);
    if (__all(f == tag)) break;
    __builtin_amdgcn_s_sleep(2);
  }
  asm volatile("" ::: "memory");
}
constexpr int g2_younger(int j) {
  int n = 0;
  if (j < 7) { n = 2 * (7 - j) + 2; for (int i = 0; i < j; ++i) n += 4; return n; }
  const int first = j < 8 ? 0 : j - 7;
  for (int i = first; i < j; ++i) n += (i + 9 < 26 ? 2 : 0) + (i + 8 < 26 ? 2 : 0);
  return n;
}

#define ATTN_INIT_ASM \
  "v_mov_b32 v32, 0\n\t" \
  "v_mov_b32 v33, 0\n\t" \
  "v_mov_b32 v34, 0\n\t" \
  "v_mov_b32 v35, 0\n\t" \
  "v_mov_b32 v36, 0\n\t" \
  "v_mov_b32 v37, 0\n\t" \
  "v_mov_b32 v38, 0\n\t" \
  "v_mov_b32 v39, 0\n\t" \
  "v_mov_b32 v40, 0\n\t" \
  "v_mov_b32 v41, 0\n\t" \
  "v_mov_b32 v42, 0\n\t" \
  "v_mov_b32 v43, 0\n\t" \
  "v_mov_b32 v44, 0\n\t" \
  "v_mov_b32 v45, 0\n\t" \
  "v_mov_b32 v46, 0\n\t" \
  "v_mov_b32 v47, 0\n\t" \
  "v_mov_b32 v48, 0\n\t" \
  "v_mov_b32 v49, 0\n\t" \
  "v_mov_b32 v50, 0\n\t" \
  "v_mov_b32 v51, 0\n\t" \
  "v_mov_b32 v52, 0\n\t" \
  "v_mov_b32 v53, 0\n\t" \
  "v_mov_b32 v54, 0\n\t" \
  "v_mov_b32 v55, 0\n\t" \
  "v_mov_b32 v56, 0\n\t" \
  "v_mov_b32 v57, 0\n\t" \
  "v_mov_b32 v58, 0\n\t" \
  "v_mov_b32 v59, 0\n\t" \
  "v_mov_b32 v60, 0\n\t" \
  "v_mov_b32 v61, 0\n\t" \
  "v_mov_b32 v62, 0\n\t" \
  "v_mov_b32 v63, 0\n\t" \
  "v_mov_b32 v64, 0\n\t" \
  "v_mov_b32 v65, 0\n\t" \
  "v_mov_b32 v66, 0\n\t" \
  "v_mov_b32 v67, 0\n\t" \
  "v_mov_b32 v68, 0\n\t" \
  "v_mov_b32 v69, 0\n\t" \
  "v_mov_b32 v70, 0\n\t" \
  "v_mov_b32 v71, 0\n\t" \
  "v_mov_b32 v72, 0\n\t" \
  "v_mov_b32 v73, 0\n\t" \
  "v_mov_b32 v74, 0\n\t" \
  "v_mov_b32 v75, 0\n\t" \
  "v_mov_b32 v76, 0\n\t" \
  "v_mov_b32 v77, 0\n\t" \
  "v_mov_b32 v78, 0\n\t" \
  "v_mov_b32 v79, 0\n\t" \
  "v_mov_b32 v80, 0\n\t" \
  "v_mov_b32 v81, 0\n\t" \
  "v_mov_b32 v82, 0\n\t" \
  "v_mov_b32 v83, 0\n\t" \
  "v_mov_b32 v84, 0\n\t" \
  "v_mov_b32 v85, 0\n\t" \
  "v_mov_b32 v86, 0\n\t" \
  "v_mov_b32 v87, 0\n\t" \
  "v_mov_b32 v88, 0\n\t" \
  "v_mov_b32 v89, 0\n\t" \
  "v_mov_b32 v90, 0\n\t" \
  "v_mov_b32 v91, 0\n\t" \
  "v_mov_b32 v92, 0\n\t" \
  "v_mov_b32 v93, 0\n\t" \
  "v_mov_b32 v94, 0\n\t" \
  "v_mov_b32 v95, 0\n\t" \
  "v_mov_b32 v96, 0\n\t" \
  "v_mov_b32 v97, 0\n\t" \
  "v_mov_b32 v98, 0\n\t" \
  "v_mov_b32 v99, 0\n\t" \
  "v_mov_b32 v100, 0\n\t" \
  "v_mov_b32 v101, 0\n\t" \
  "v_mov_b32 v102, 0\n\t" \
  "v_mov_b32 v103, 0\n\t" \
  "v_mov_b32 v104, 0\n\t" \
  "v_mov_b32 v105, 0\n\t" \
  "v_mov_b32 v106, 0\n\t" \
  "v_mov_b32 v107, 0\n\t" \
  "v_mov_b32 v108, 0\n\t" \
  "v_mov_b32 v109, 0\n\t" \
  "v_mov_b32 v110, 0\n\t" \
  "v_mov_b32 v111, 0\n\t" \
  "v_mov_b32 v112, 0\n\t" \
  "v_mov_b32 v113, 0\n\t" \
  "v_mov_b32 v114, 0\n\t" \
  "v_mov_b32 v115, 0\n\t" \
  "v_mov_b32 v116, 0\n\t" \
  "v_mov_b32 v117, 0\n\t" \
  "v_mov_b32 v118, 0\n\t" \
  "v_mov_b32 v119, 0\n\t" \
  "v_mov_b32 v120, 0\n\t" \
  "v_mov_b32 v121, 0\n\t" \
  "v_mov_b32 v122, 0\n\t" \
  "v_mov_b32 v123, 0\n\t" \
  "v_mov_b32 v124, 0\n\t" \
  "v_mov_b32 v125, 0\n\t" \
  "v_mov_b32 v126, 0\n\t" \
  "v_mov_b32 v127, 0\n\t" \
  "v_mov_b32 v128, 0\n\t" \
  "v_mov_b32 v129, 0\n\t" \
  "v_mov_b32 v130, 0\n\t" \
  "v_mov_b32 v131, 0\n\t" \
  "v_mov_b32 v132, 0\n\t" \
  "v_mov_b32 v133, 0\n\t" \
  "v_mov_b32 v134, 0\n\t" \
  "v_mov_b32 v135, 0\n\t" \
  "v_mov_b32 v136, 0\n\t" \
  "v_mov_b32 v137, 0\n\t" \
  "v_mov_b32 v138, 0\n\t" \
  "v_mov_b32 v139, 0\n\t" \
  "v_mov_b32 v140, 0\n\t" \
  "v_mov_b32 v141, 0\n\t" \
  "v_mov_b32 v142, 0\n\t" \
  "v_mov_b32 v143, 0\n\t" \
  "v_mov_b32 v144, 0\n\t" \
  "v_mov_b32 v145, 0\n\t" \
  "v_mov_b32 v146, 0\n\t" \
  "v_mov_b32 v147, 0\n\t" \
  "v_mov_b32 v148, 0\n\t" \
  "v_mov_b32 v149, 0\n\t" \
  "v_mov_b32 v150, 0\n\t" \
  "v_mov_b32 v151, 0\n\t" \
  "v_mov_b32 v152, 0\n\t" \
  "v_mov_b32 v153, 0\n\t" \
  "v_mov_b32 v154, 0\n\t" \
  "v_mov_b32 v155, 0\n\t" \
  "v_mov_b32 v156, 0\n\t" \
  "v_mov_b32 v157, 0\n\t" \
  "v_mov_b32 v158, 0\n\t" \
  "v_mov_b32 v159, 0\n\t" \
  ""
#define ATTN_FIRST_ASM \
  "v_mov_b32 v232, %[vc]\n\t" \
  "v_mov_b32 v233, %[vc]\n\t" \
  "v_mov_b32 v234, %[vc]\n\t" \
  "v_mov_b32 v235, %[vc]\n\t" \
  "v_mov_b32 v236, %[vc]\n\t" \
  "v_mov_b32 v237, %[vc]\n\t" \
  "v_mov_b32 v238, %[vc]\n\t" \
  "v_mov_b32 v239, %[vc]\n\t" \
  "v_mov_b32 v240, %[vc]\n\t" \
  "v_mov_b32 v241, %[vc]\n\t" \
  "v_mov_b32 v242, %[vc]\n\t" \
  "v_mov_b32 v243, %[vc]\n\t" \
  "v_mov_b32 v244, %[vc]\n\t" \
  "v_mov_b32 v245, %[vc]\n\t" \
  "v_mov_b32 v246, %[vc]\n\t" \
  "v_mov_b32 v247, %[vc]\n\t" \
  "5:\n\t" \
  "s_waitcnt vmcnt(0)\n\t" \
  "v_cmp_ne_u32 vcc, %[tag], %[f]\n\t" \
  "s_cbranch_vccz 6f\n\t" \
  "buffer_load_dword %[f], %[fo], %[rs], 0 offen sc1\n\t" \
  "s_sub_u32 %[cap], %[cap], 1\n\t" \
  "s_cbranch_scc0 5b\n\t" \
  "s_waitcnt vmcnt(0)\n\t" \
  "6:\n\t" \
  "buffer_load_dwordx4 v[224:227], v248, %[rs], %[so] offen offset:0 sc1\n\t" \
  "s_mov_b64 exec, %[vm]\n\t" \
  "buffer_load_dwordx4 v[232:235], v249, %[rs], %[so] offen offset:1024 sc1\n\t" \
  "buffer_load_dwordx4 v[236:239], v249, %[rs], %[so] offen offset:1536 sc1\n\t" \
  "s_mov_b64 exec, -1\n\t" \
  "buffer_load_dwordx4 v[228:231], v248, %[rs], %[so] offen offset:2048 sc1\n\t" \
  "s_mov_b64 exec, %[vm]\n\t" \
  "buffer_load_dwordx4 v[240:243], v249, %[rs], %[so] offen offset:3072 sc1\n\t" \
  "buffer_load_dwordx4 v[244:247], v249, %[rs], %[so] offen offset:3584 sc1\n\t" \
  "s_mov_b64 exec, -1\n\t" \
  ""
#define ATTN_MAIN_ASM \
  "s_add_u32 %[t], %[q0], 147456\n\t" \
  "v_add_u32 v192, %[t], v248\n\t" \
  "s_add_u32 %[t], %[q1], 147456\n\t" \
  "v_add_u32 v193, %[t], v248\n\t" \
  "s_add_u32 %[t], %[q2], 147456\n\t" \
  "v_add_u32 v194, %[t], v248\n\t" \
  "s_add_u32 %[t], %[q3], 147456\n\t" \
  "v_add_u32 v195, %[t], v248\n\t" \
  "ds_read_b128 v[160:163], v192 offset:0\n\t" \
  "ds_read_b128 v[164:167], v192 offset:1024\n\t" \
  "ds_read_b128 v[168:171], v193 offset:0\n\t" \
  "ds_read_b128 v[172:175], v193 offset:1024\n\t" \
  "ds_read_b128 v[176:179], v194 offset:0\n\t" \
  "ds_read_b128 v[180:183], v194 offset:1024\n\t" \
  "ds_read_b128 v[184:187], v195 offset:0\n\t" \
  "ds_read_b128 v[188:191], v195 offset:1024\n\t" \
  "v_mov_b32 v250, 0xdddd\n\t" \
  "s_waitcnt vmcnt(3) lgkmcnt(7)\n\t" \
  "v_mfma_f32_32x32x16_bf16 v[192:207], v[224:227], v[160:163], 0\n\t" \
  "s_nop 10\n\t" \
  "s_waitcnt lgkmcnt(0)\n\t" \
  "v_exp_f32 v192, v192\n\t" \
  "v_exp_f32 v193, v193\n\t" \
  "s_branch 4f\n\t" \
  ".p2align 6\n\t" \
  "1:\n\t" \
  "v_exp_f32 v192, v192\n\t" \
  "v_exp_f32 v193, v193\n\t" \
  "v_smfmac_f32_32x32x32_bf16 v[144:159], v[244:247], v[216:223], v250\n\t" \
  "buffer_load_dwordx4 v[228:231], v248, %[rs], %[so] offen offset:2048 sc1\n\t" \
  "s_mov_b64 exec, %[vm]\n\t" \
  "buffer_load_dwordx4 v[240:243], v249, %[rs], %[so] offen offset:3072 sc1\n\t" \
  "buffer_load_dwordx4 v[244:247], v249, %[rs], %[so] offen offset:3584 sc1\n\t" \
  "s_mov_b64 exec, -1\n\t" \
  "4:\n\t" \
  "v_exp_f32 v194, v194\n\t" \
  "v_exp_f32 v195, v195\n\t" \
  "v_exp_f32 v196, v196\n\t" \
  "v_exp_f32 v197, v197\n\t" \
  "v_exp_f32 v198, v198\n\t" \
  "v_mfma_f32_32x32x16_bf16 v[208:223], v[224:227], v[164:167], 0\n\t" \
  "v_exp_f32 v199, v199\n\t" \
  "v_exp_f32 v200, v200\n\t" \
  "v_exp_f32 v201, v201\n\t" \
  "v_exp_f32 v202, v202\n\t" \
  "v_exp_f32 v203, v203\n\t" \
  "v_smfmac_f32_32x32x32_bf16 v[32:47], v[232:235], v[192:199], v250\n\t" \
  "v_exp_f32 v204, v204\n\t" \
  "v_exp_f32 v205, v205\n\t" \
  "v_exp_f32 v206, v206\n\t" \
  "v_exp_f32 v207, v207\n\t" \
  "v_exp_f32 v208, v208\n\t" \
  "v_exp_f32 v209, v209\n\t" \
  "v_smfmac_f32_32x32x32_bf16 v[32:47], v[236:239], v[200:207], v250\n\t" \
  "v_exp_f32 v210, v210\n\t" \
  "v_exp_f32 v211, v211\n\t" \
  "v_exp_f32 v212, v212\n\t" \
  "v_exp_f32 v213, v213\n\t" \
  "v_exp_f32 v214, v214\n\t" \
  "v_mfma_f32_32x32x16_bf16 v[192:207], v[224:227], v[168:171], 0\n\t" \
  "v_exp_f32 v215, v215\n\t" \
  "v_exp_f32 v216, v216\n\t" \
  "v_exp_f32 v217, v217\n\t" \
  "v_exp_f32 v218, v218\n\t" \
  "v_exp_f32 v219, v219\n\t" \
  "v_smfmac_f32_32x32x32_bf16 v[48:63], v[232:235], v[208:215], v250\n\t" \
  "v_exp_f32 v220, v220\n\t" \
  "v_exp_f32 v221, v221\n\t" \
  "v_exp_f32 v222, v222\n\t" \
  "v_exp_f32 v223, v223\n\t" \
  "v_exp_f32 v192, v192\n\t" \
  "v_exp_f32 v193, v193\n\t" \
  "v_smfmac_f32_32x32x32_bf16 v[48:63], v[236:239], v[216:223], v250\n\t" \
  "v_exp_f32 v194, v194\n\t" \
  "v_exp_f32 v195, v195\n\t" \
  "v_exp_f32 v196, v196\n\t" \
  "v_exp_f32 v197, v197\n\t" \
  "v_exp_f32 v198, v198\n\t" \
  "v_mfma_f32_32x32x16_bf16 v[208:223], v[224:227], v[172:175], 0\n\t" \
  "v_exp_f32 v199, v199\n\t" \
  "v_exp_f32 v200, v200\n\t" \
  "v_exp_f32 v201, v201\n\t" \
  "v_exp_f32 v202, v202\n\t" \
  "v_exp_f32 v203, v203\n\t" \
  "v_smfmac_f32_32x32x32_bf16 v[64:79], v[232:235], v[192:199], v250\n\t" \
  "v_exp_f32 v204, v204\n\t" \
  "v_exp_f32 v205, v205\n\t" \
  "v_exp_f32 v206, v206\n\t" \
  "v_exp_f32 v207, v207\n\t" \
  "v_exp_f32 v208, v208\n\t" \
  "v_exp_f32 v209, v209\n\t" \
  "v_smfmac_f32_32x32x32_bf16 v[64:79], v[236:239], v[200:207], v250\n\t" \
  "v_exp_f32 v210, v210\n\t" \
  "v_exp_f32 v211, v211\n\t" \
  "v_exp_f32 v212, v212\n\t" \
  "v_exp_f32 v213, v213\n\t" \
  "v_exp_f32 v214, v214\n\t" \
  "v_mfma_f32_32x32x16_bf16 v[192:207], v[224:227], v[176:179], 0\n\t" \
  "v_exp_f32 v215, v215\n\t" \
  "v_exp_f32 v216, v216\n\t" \
  "v_exp_f32 v217, v217\n\t" \
  "v_exp_f32 v218, v218\n\t" \
  "v_exp_f32 v219, v219\n\t" \
  "v_smfmac_f32_32x32x32_bf16 v[80:95], v[232:235], v[208:215], v250\n\t" \
  "v_exp_f32 v220, v220\n\t" \
  "v_exp_f32 v221, v221\n\t" \
  "v_exp_f32 v222, v222\n\t" \
  "v_exp_f32 v223, v223\n\t" \
  "v_exp_f32 v192, v192\n\t" \
  "v_exp_f32 v193, v193\n\t" \
  "v_smfmac_f32_32x32x32_bf16 v[80:95], v[236:239], v[216:223], v250\n\t" \
  "s_add_u32 %[so], %[so], 4096\n\t" \
  "s_and_b32 %[so], %[so], 0x7fff\n\t" \
  "s_or_b32 %[so], %[so], %[sb]\n\t" \
  "v_exp_f32 v194, v194\n\t" \
  "v_exp_f32 v195, v195\n\t" \
  "v_exp_f32 v196, v196\n\t" \
  "v_exp_f32 v197, v197\n\t" \
  "v_exp_f32 v198, v198\n\t" \
  "v_mfma_f32_32x32x16_bf16 v[208:223], v[224:227], v[180:183], 0\n\t" \
  "v_exp_f32 v199, v199\n\t" \
  "v_exp_f32 v200, v200\n\t" \
  "v_exp_f32 v201, v201\n\t" \
  "v_exp_f32 v202, v202\n\t" \
  "v_exp_f32 v203, v203\n\t" \
  "v_smfmac_f32_32x32x32_bf16 v[96:111], v[232:235], v[192:199], v250\n\t" \
  "v_exp_f32 v204, v204\n\t" \
  "v_exp_f32 v205, v205\n\t" \
  "v_exp_f32 v206, v206\n\t" \
  "v_exp_f32 v207, v207\n\t" \
  "v_exp_f32 v208, v208\n\t" \
  "v_exp_f32 v209, v209\n\t" \
  "v_smfmac_f32_32x32x32_bf16 v[96:111], v[236:239], v[200:207], v250\n\t" \
  "v_exp_f32 v210, v210\n\t" \
  "v_exp_f32 v211, v211\n\t" \
  "v_exp_f32 v212, v212\n\t" \
  "v_exp_f32 v213, v213\n\t" \
  "v_exp_f32 v214, v214\n\t" \
  "v_mfma_f32_32x32x16_bf16 v[192:207], v[224:227], v[184:187], 0\n\t" \
  "v_exp_f32 v215, v215\n\t" \
  "v_exp_f32 v216, v216\n\t" \
  "v_exp_f32 v217, v217\n\t" \
  "v_exp_f32 v218, v218\n\t" \
  "v_exp_f32 v219, v219\n\t" \
  "v_smfmac_f32_32x32x32_bf16 v[112:127], v[232:235], v[208:215], v250\n\t" \
  "v_exp_f32 v220, v220\n\t" \
  "v_exp_f32 v221, v221\n\t" \
  "v_exp_f32 v222, v222\n\t" \
  "v_exp_f32 v223, v223\n\t" \
  "v_exp_f32 v192, v192\n\t" \
  "v_exp_f32 v193, v193\n\t" \
  "v_smfmac_f32_32x32x32_bf16 v[112:127], v[236:239], v[216:223], v250\n\t" \
  "v_exp_f32 v194, v194\n\t" \
  "v_exp_f32 v195, v195\n\t" \
  "v_exp_f32 v196, v196\n\t" \
  "v_exp_f32 v197, v197\n\t" \
  "v_exp_f32 v198, v198\n\t" \
  "v_mfma_f32_32x32x16_bf16 v[208:223], v[224:227], v[188:191], 0\n\t" \
  "v_exp_f32 v199, v199\n\t" \
  "v_exp_f32 v200, v200\n\t" \
  "v_exp_f32 v201, v201\n\t" \
  "v_exp_f32 v202, v202\n\t" \
  "v_exp_f32 v203, v203\n\t" \
  "v_smfmac_f32_32x32x32_bf16 v[128:143], v[232:235], v[192:199], v250\n\t" \
  "v_exp_f32 v204, v204\n\t" \
  "v_exp_f32 v205, v205\n\t" \
  "v_exp_f32 v206, v206\n\t" \
  "v_exp_f32 v207, v207\n\t" \
  "v_exp_f32 v208, v208\n\t" \
  "v_exp_f32 v209, v209\n\t" \
  "v_smfmac_f32_32x32x32_bf16 v[128:143], v[236:239], v[200:207], v250\n\t" \
  "v_exp_f32 v210, v210\n\t" \
  "v_exp_f32 v211, v211\n\t" \
  "v_exp_f32 v212, v212\n\t" \
  "v_exp_f32 v213, v213\n\t" \
  "v_exp_f32 v214, v214\n\t" \
  "s_waitcnt vmcnt(0)\n\t" \
  "v_mfma_f32_32x32x16_bf16 v[192:207], v[228:231], v[160:163], 0\n\t" \
  "v_exp_f32 v215, v215\n\t" \
  "v_exp_f32 v216, v216\n\t" \
  "v_exp_f32 v217, v217\n\t" \
  "v_exp_f32 v218, v218\n\t" \
  "v_exp_f32 v219, v219\n\t" \
  "v_smfmac_f32_32x32x32_bf16 v[144:159], v[232:235], v[208:215], v250\n\t" \
  "v_exp_f32 v220, v220\n\t" \
  "v_exp_f32 v221, v221\n\t" \
  "v_exp_f32 v222, v222\n\t" \
  "v_exp_f32 v223, v223\n\t" \
  "v_exp_f32 v192, v192\n\t" \
  "v_exp_f32 v193, v193\n\t" \
  "v_smfmac_f32_32x32x32_bf16 v[144:159], v[236:239], v[216:223], v250\n\t" \
  "buffer_load_dwordx4 v[224:227], v248, %[rs], %[so] offen offset:0 sc1\n\t" \
  "s_mov_b64 exec, %[vm]\n\t" \
  "buffer_load_dwordx4 v[232:235], v249, %[rs], %[so] offen offset:1024 sc1\n\t" \
  "buffer_load_dwordx4 v[236:239], v249, %[rs], %[so] offen offset:1536 sc1\n\t" \
  "s_mov_b64 exec, -1\n\t" \
  "v_exp_f32 v194, v194\n\t" \
  "v_exp_f32 v195, v195\n\t" \
  "v_exp_f32 v196, v196\n\t" \
  "v_exp_f32 v197, v197\n\t" \
  "v_exp_f32 v198, v198\n\t" \
  "v_mfma_f32_32x32x16_bf16 v[208:223], v[228:231], v[164:167], 0\n\t" \
  "v_exp_f32 v199, v199\n\t" \
  "v_exp_f32 v200, v200\n\t" \
  "v_exp_f32 v201, v201\n\t" \
  "v_exp_f32 v202, v202\n\t" \
  "v_exp_f32 v203, v203\n\t" \
  "v_smfmac_f32_32x32x32_bf16 v[32:47], v[240:243], v[192:199], v250\n\t" \
  "v_exp_f32 v204, v204\n\t" \
  "v_exp_f32 v205, v205\n\t" \
  "v_exp_f32 v206, v206\n\t" \
  "v_exp_f32 v207, v207\n\t" \
  "v_exp_f32 v208, v208\n\t" \
  "v_exp_f32 v209, v209\n\t" \
  "v_smfmac_f32_32x32x32_bf16 v[32:47], v[244:247], v[200:207], v250\n\t" \
  "v_exp_f32 v210, v210\n\t" \
  "v_exp_f32 v211, v211\n\t" \
  "v_exp_f32 v212, v212\n\t" \
  "v_exp_f32 v213, v213\n\t" \
  "v_exp_f32 v214, v214\n\t" \
  "v_mfma_f32_32x32x16_bf16 v[192:207], v[228:231], v[168:171], 0\n\t" \
  "v_exp_f32 v215, v215\n\t" \
  "v_exp_f32 v216, v216\n\t" \
  "v_exp_f32 v217, v217\n\t" \
  "v_exp_f32 v218, v218\n\t" \
  "v_exp_f32 v219, v219\n\t" \
  "v_smfmac_f32_32x32x32_bf16 v[48:63], v[240:243], v[208:215], v250\n\t" \
  "v_exp_f32 v220, v220\n\t" \
  "v_exp_f32 v221, v221\n\t" \
  "v_exp_f32 v222, v222\n\t" \
  "v_exp_f32 v223, v223\n\t" \
  "v_exp_f32 v192, v192\n\t" \
  "v_exp_f32 v193, v193\n\t" \
  "v_smfmac_f32_32x32x32_bf16 v[48:63], v[244:247], v[216:223], v250\n\t" \
  "v_exp_f32 v194, v194\n\t" \
  "v_exp_f32 v195, v195\n\t" \
  "v_exp_f32 v196, v196\n\t" \
  "v_exp_f32 v197, v197\n\t" \
  "v_exp_f32 v198, v198\n\t" \
  "v_mfma_f32_32x32x16_bf16 v[208:223], v[228:231], v[172:175], 0\n\t" \
  "v_exp_f32 v199, v199\n\t" \
  "v_exp_f32 v200, v200\n\t" \
  "v_exp_f32 v201, v201\n\t" \
  "v_exp_f32 v202, v202\n\t" \
  "v_exp_f32 v203, v203\n\t" \
  "v_smfmac_f32_32x32x32_bf16 v[64:79], v[240:243], v[192:199], v250\n\t" \
  "v_exp_f32 v204, v204\n\t" \
  "v_exp_f32 v205, v205\n\t" \
  "v_exp_f32 v206, v206\n\t" \
  "v_exp_f32 v207, v207\n\t" \
  "v_exp_f32 v208, v208\n\t" \
  "v_exp_f32 v209, v209\n\t" \
  "v_smfmac_f32_32x32x32_bf16 v[64:79], v[244:247], v[200:207], v250\n\t" \
  "v_exp_f32 v210, v210\n\t" \
  "v_exp_f32 v211, v211\n\t" \
  "v_exp_f32 v212, v212\n\t" \
  "v_exp_f32 v213, v213\n\t" \
  "v_exp_f32 v214, v214\n\t" \
  "v_mfma_f32_32x32x16_bf16 v[192:207], v[228:231], v[176:179], 0\n\t" \
  "v_exp_f32 v215, v215\n\t" \
  "v_exp_f32 v216, v216\n\t" \
  "v_exp_f32 v217, v217\n\t" \
  "v_exp_f32 v218, v218\n\t" \
  "v_exp_f32 v219, v219\n\t" \
  "v_smfmac_f32_32x32x32_bf16 v[80:95], v[240:243], v[208:215], v250\n\t" \
  "v_exp_f32 v220, v220\n\t" \
  "v_exp_f32 v221, v221\n\t" \
  "v_exp_f32 v222, v222\n\t" \
  "v_exp_f32 v223, v223\n\t" \
  "v_exp_f32 v192, v192\n\t" \
  "v_exp_f32 v193, v193\n\t" \
  "v_smfmac_f32_32x32x32_bf16 v[80:95], v[244:247], v[216:223], v250\n\t" \
  "v_exp_f32 v194, v194\n\t" \
  "v_exp_f32 v195, v195\n\t" \
  "v_exp_f32 v196, v196\n\t" \
  "v_exp_f32 v197, v197\n\t" \
  "v_exp_f32 v198, v198\n\t" \
  "v_mfma_f32_32x32x16_bf16 v[208:223], v[228:231], v[180:183], 0\n\t" \
  "v_exp_f32 v199, v199\n\t" \
  "v_exp_f32 v200, v200\n\t" \
  "v_exp_f32 v201, v201\n\t" \
  "v_exp_f32 v202, v202\n\t" \
  "v_exp_f32 v203, v203\n\t" \
  "v_smfmac_f32_32x32x32_bf16 v[96:111], v[240:243], v[192:199], v250\n\t" \
  "v_exp_f32 v204, v204\n\t" \
  "v_exp_f32 v205, v205\n\t" \
  "v_exp_f32 v206, v206\n\t" \
  "v_exp_f32 v207, v207\n\t" \
  "v_exp_f32 v208, v208\n\t" \
  "v_exp_f32 v209, v209\n\t" \
  "v_smfmac_f32_32x32x32_bf16 v[96:111], v[244:247], v[200:207], v250\n\t" \
  "v_exp_f32 v210, v210\n\t" \
  "v_exp_f32 v211, v211\n\t" \
  "v_exp_f32 v212, v212\n\t" \
  "v_exp_f32 v213, v213\n\t" \
  "v_exp_f32 v214, v214\n\t" \
  "v_mfma_f32_32x32x16_bf16 v[192:207], v[228:231], v[184:187], 0\n\t" \
  "v_exp_f32 v215, v215\n\t" \
  "v_exp_f32 v216, v216\n\t" \
  "v_exp_f32 v217, v217\n\t" \
  "v_exp_f32 v218, v218\n\t" \
  "v_exp_f32 v219, v219\n\t" \
  "v_smfmac_f32_32x32x32_bf16 v[112:127], v[240:243], v[208:215], v250\n\t" \
  "v_exp_f32 v220, v220\n\t" \
  "v_exp_f32 v221, v221\n\t" \
  "v_exp_f32 v222, v222\n\t" \
  "v_exp_f32 v223, v223\n\t" \
  "v_exp_f32 v192, v192\n\t" \
  "v_exp_f32 v193, v193\n\t" \
  "v_smfmac_f32_32x32x32_bf16 v[112:127], v[244:247], v[216:223], v250\n\t" \
  "v_exp_f32 v194, v194\n\t" \
  "v_exp_f32 v195, v195\n\t" \
  "v_exp_f32 v196, v196\n\t" \
  "v_exp_f32 v197, v197\n\t" \
  "v_exp_f32 v198, v198\n\t" \
  "v_mfma_f32_32x32x16_bf16 v[208:223], v[228:231], v[188:191], 0\n\t" \
  "v_exp_f32 v199, v199\n\t" \
  "v_exp_f32 v200, v200\n\t" \
  "v_exp_f32 v201, v201\n\t" \
  "v_exp_f32 v202, v202\n\t" \
  "v_exp_f32 v203, v203\n\t" \
  "v_smfmac_f32_32x32x32_bf16 v[128:143], v[240:243], v[192:199], v250\n\t" \
  "v_exp_f32 v204, v204\n\t" \
  "v_exp_f32 v205, v205\n\t" \
  "v_exp_f32 v206, v206\n\t" \
  "v_exp_f32 v207, v207\n\t" \
  "v_exp_f32 v208, v208\n\t" \
  "v_exp_f32 v209, v209\n\t" \
  "v_smfmac_f32_32x32x32_bf16 v[128:143], v[244:247], v[200:207], v250\n\t" \
  "v_exp_f32 v210, v210\n\t" \
  "v_exp_f32 v211, v211\n\t" \
  "v_exp_f32 v212, v212\n\t" \
  "v_exp_f32 v213, v213\n\t" \
  "v_exp_f32 v214, v214\n\t" \
  "s_waitcnt vmcnt(0)\n\t" \
  "v_mfma_f32_32x32x16_bf16 v[192:207], v[224:227], v[160:163], 0\n\t" \
  "v_exp_f32 v215, v215\n\t" \
  "v_exp_f32 v216, v216\n\t" \
  "v_exp_f32 v217, v217\n\t" \
  "v_exp_f32 v218, v218\n\t" \
  "v_exp_f32 v219, v219\n\t" \
  "v_smfmac_f32_32x32x32_bf16 v[144:159], v[240:243], v[208:215], v250\n\t" \
  "s_sub_u32 %[cnt], %[cnt], 1\n\t" \
  "s_cmp_lg_u32 %[cnt], 0\n\t" \
  "v_exp_f32 v220, v220\n\t" \
  "v_exp_f32 v221, v221\n\t" \
  "v_exp_f32 v222, v222\n\t" \
  "v_exp_f32 v223, v223\n\t" \
  "s_cbranch_scc1 1b\n\t" \
  ".Lattn_last:\n\t" \
  "v_exp_f32 v192, v192\n\t" \
  "v_exp_f32 v193, v193\n\t" \
  "v_smfmac_f32_32x32x32_bf16 v[144:159], v[244:247], v[216:223], v250\n\t" \
  "buffer_load_dwordx4 v[228:231], v248, %[rs], %[so] offen offset:2048 sc1\n\t" \
  "s_mov_b64 exec, %[vm]\n\t" \
  "buffer_load_dwordx4 v[240:243], v249, %[rs], %[so] offen offset:3072 sc1\n\t" \
  "buffer_load_dwordx4 v[244:247], v249, %[rs], %[so] offen offset:3584 sc1\n\t" \
  "s_mov_b64 exec, -1\n\t" \
  "v_exp_f32 v194, v194\n\t" \
  "v_exp_f32 v195, v195\n\t" \
  "v_exp_f32 v196, v196\n\t" \
  "v_exp_f32 v197, v197\n\t" \
  "v_exp_f32 v198, v198\n\t" \
  "v_mfma_f32_32x32x16_bf16 v[208:223], v[224:227], v[164:167], 0\n\t" \
  "v_exp_f32 v199, v199\n\t" \
  "v_exp_f32 v200, v200\n\t" \
  "v_exp_f32 v201, v201\n\t" \
  "v_exp_f32 v202, v202\n\t" \
  "v_exp_f32 v203, v203\n\t" \
  "v_smfmac_f32_32x32x32_bf16 v[32:47], v[232:235], v[192:199], v250\n\t" \
  "v_exp_f32 v204, v204\n\t" \
  "v_exp_f32 v205, v205\n\t" \
  "v_exp_f32 v206, v206\n\t" \
  "v_exp_f32 v207, v207\n\t" \
  "v_exp_f32 v208, v208\n\t" \
  "v_exp_f32 v209, v209\n\t" \
  "v_smfmac_f32_32x32x32_bf16 v[32:47], v[236:239], v[200:207], v250\n\t" \
  "v_exp_f32 v210, v210\n\t" \
  "v_exp_f32 v211, v211\n\t" \
  "v_exp_f32 v212, v212\n\t" \
  "v_exp_f32 v213, v213\n\t" \
  "v_exp_f32 v214, v214\n\t" \
  "v_mfma_f32_32x32x16_bf16 v[192:207], v[224:227], v[168:171], 0\n\t" \
  "v_exp_f32 v215, v215\n\t" \
  "v_exp_f32 v216, v216\n\t" \
  "v_exp_f32 v217, v217\n\t" \
  "v_exp_f32 v218, v218\n\t" \
  "v_exp_f32 v219, v219\n\t" \
  "v_smfmac_f32_32x32x32_bf16 v[48:63], v[232:235], v[208:215], v250\n\t" \
  "v_exp_f32 v220, v220\n\t" \
  "v_exp_f32 v221, v221\n\t" \
  "v_exp_f32 v222, v222\n\t" \
  "v_exp_f32 v223, v223\n\t" \
  "v_exp_f32 v192, v192\n\t" \
  "v_exp_f32 v193, v193\n\t" \
  "v_smfmac_f32_32x32x32_bf16 v[48:63], v[236:239], v[216:223], v250\n\t" \
  "v_exp_f32 v194, v194\n\t" \
  "v_exp_f32 v195, v195\n\t" \
  "v_exp_f32 v196, v196\n\t" \
  "v_exp_f32 v197, v197\n\t" \
  "v_exp_f32 v198, v198\n\t" \
  "v_mfma_f32_32x32x16_bf16 v[208:223], v[224:227], v[172:175], 0\n\t" \
  "v_exp_f32 v199, v199\n\t" \
  "v_exp_f32 v200, v200\n\t" \
  "v_exp_f32 v201, v201\n\t" \
  "v_exp_f32 v202, v202\n\t" \
  "v_exp_f32 v203, v203\n\t" \
  "v_smfmac_f32_32x32x32_bf16 v[64:79], v[232:235], v[192:199], v250\n\t" \
  "v_exp_f32 v204, v204\n\t" \
  "v_exp_f32 v205, v205\n\t" \
  "v_exp_f32 v206, v206\n\t" \
  "v_exp_f32 v207, v207\n\t" \
  "v_exp_f32 v208, v208\n\t" \
  "v_exp_f32 v209, v209\n\t" \
  "v_smfmac_f32_32x32x32_bf16 v[64:79], v[236:239], v[200:207], v250\n\t" \
  "v_exp_f32 v210, v210\n\t" \
  "v_exp_f32 v211, v211\n\t" \
  "v_exp_f32 v212, v212\n\t" \
  "v_exp_f32 v213, v213\n\t" \
  "v_exp_f32 v214, v214\n\t" \
  "v_mfma_f32_32x32x16_bf16 v[192:207], v[224:227], v[176:179], 0\n\t" \
  "v_exp_f32 v215, v215\n\t" \
  "v_exp_f32 v216, v216\n\t" \
  "v_exp_f32 v217, v217\n\t" \
  "v_exp_f32 v218, v218\n\t" \
  "v_exp_f32 v219, v219\n\t" \
  "v_smfmac_f32_32x32x32_bf16 v[80:95], v[232:235], v[208:215], v250\n\t" \
  "v_exp_f32 v220, v220\n\t" \
  "v_exp_f32 v221, v221\n\t" \
  "v_exp_f32 v222, v222\n\t" \
  "v_exp_f32 v223, v223\n\t" \
  "v_exp_f32 v192, v192\n\t" \
  "v_exp_f32 v193, v193\n\t" \
  "v_smfmac_f32_32x32x32_bf16 v[80:95], v[236:239], v[216:223], v250\n\t" \
  "v_exp_f32 v194, v194\n\t" \
  "v_exp_f32 v195, v195\n\t" \
  "v_exp_f32 v196, v196\n\t" \
  "v_exp_f32 v197, v197\n\t" \
  "v_exp_f32 v198, v198\n\t" \
  "v_mfma_f32_32x32x16_bf16 v[208:223], v[224:227], v[180:183], 0\n\t" \
  "v_exp_f32 v199, v199\n\t" \
  "v_exp_f32 v200, v200\n\t" \
  "v_exp_f32 v201, v201\n\t" \
  "v_exp_f32 v202, v202\n\t" \
  "v_exp_f32 v203, v203\n\t" \
  "v_smfmac_f32_32x32x32_bf16 v[96:111], v[232:235], v[192:199], v250\n\t" \
  "v_exp_f32 v204, v204\n\t" \
  "v_exp_f32 v205, v205\n\t" \
  "v_exp_f32 v206, v206\n\t" \
  "v_exp_f32 v207, v207\n\t" \
  "v_exp_f32 v208, v208\n\t" \
  "v_exp_f32 v209, v209\n\t" \
  "v_smfmac_f32_32x32x32_bf16 v[96:111], v[236:239], v[200:207], v250\n\t" \
  "v_exp_f32 v210, v210\n\t" \
  "v_exp_f32 v211, v211\n\t" \
  "v_exp_f32 v212, v212\n\t" \
  "v_exp_f32 v213, v213\n\t" \
  "v_exp_f32 v214, v214\n\t" \
  "v_mfma_f32_32x32x16_bf16 v[192:207], v[224:227], v[184:187], 0\n\t" \
  "v_exp_f32 v215, v215\n\t" \
  "v_exp_f32 v216, v216\n\t" \
  "v_exp_f32 v217, v217\n\t" \
  "v_exp_f32 v218, v218\n\t" \
  "v_exp_f32 v219, v219\n\t" \
  "v_smfmac_f32_32x32x32_bf16 v[112:127], v[232:235], v[208:215], v250\n\t" \
  "v_exp_f32 v220, v220\n\t" \
  "v_exp_f32 v221, v221\n\t" \
  "v_exp_f32 v222, v222\n\t" \
  "v_exp_f32 v223, v223\n\t" \
  "v_exp_f32 v192, v192\n\t" \
  "v_exp_f32 v193, v193\n\t" \
  "v_smfmac_f32_32x32x32_bf16 v[112:127], v[236:239], v[216:223], v250\n\t" \
  "v_exp_f32 v194, v194\n\t" \
  "v_exp_f32 v195, v195\n\t" \
  "v_exp_f32 v196, v196\n\t" \
  "v_exp_f32 v197, v197\n\t" \
  "v_exp_f32 v198, v198\n\t" \
  "v_mfma_f32_32x32x16_bf16 v[208:223], v[224:227], v[188:191], 0\n\t" \
  "v_exp_f32 v199, v199\n\t" \
  "v_exp_f32 v200, v200\n\t" \
  "v_exp_f32 v201, v201\n\t" \
  "v_exp_f32 v202, v202\n\t" \
  "v_exp_f32 v203, v203\n\t" \
  "v_smfmac_f32_32x32x32_bf16 v[128:143], v[232:235], v[192:199], v250\n\t" \
  "v_exp_f32 v204, v204\n\t" \
  "v_exp_f32 v205, v205\n\t" \
  "v_exp_f32 v206, v206\n\t" \
  "v_exp_f32 v207, v207\n\t" \
  "v_exp_f32 v208, v208\n\t" \
  "v_exp_f32 v209, v209\n\t" \
  "v_smfmac_f32_32x32x32_bf16 v[128:143], v[236:239], v[200:207], v250\n\t" \
  "v_exp_f32 v210, v210\n\t" \
  "v_exp_f32 v211, v211\n\t" \
  "v_exp_f32 v212, v212\n\t" \
  "v_exp_f32 v213, v213\n\t" \
  "v_exp_f32 v214, v214\n\t" \
  "s_waitcnt vmcnt(0)\n\t" \
  "v_mfma_f32_32x32x16_bf16 v[192:207], v[228:231], v[160:163], 0\n\t" \
  "v_exp_f32 v215, v215\n\t" \
  "v_exp_f32 v216, v216\n\t" \
  "v_exp_f32 v217, v217\n\t" \
  "v_exp_f32 v218, v218\n\t" \
  "v_exp_f32 v219, v219\n\t" \
  "v_smfmac_f32_32x32x32_bf16 v[144:159], v[232:235], v[208:215], v250\n\t" \
  "v_exp_f32 v220, v220\n\t" \
  "v_exp_f32 v221, v221\n\t" \
  "v_exp_f32 v222, v222\n\t" \
  "v_exp_f32 v223, v223\n\t" \
  "v_exp_f32 v192, v192\n\t" \
  "v_exp_f32 v193, v193\n\t" \
  "v_smfmac_f32_32x32x32_bf16 v[144:159], v[236:239], v[216:223], v250\n\t" \
  "v_exp_f32 v194, v194\n\t" \
  "v_exp_f32 v195, v195\n\t" \
  "v_exp_f32 v196, v196\n\t" \
  "v_exp_f32 v197, v197\n\t" \
  "v_exp_f32 v198, v198\n\t" \
  "v_mfma_f32_32x32x16_bf16 v[208:223], v[228:231], v[164:167], 0\n\t" \
  "v_exp_f32 v199, v199\n\t" \
  "v_exp_f32 v200, v200\n\t" \
  "v_exp_f32 v201, v201\n\t" \
  "v_exp_f32 v202, v202\n\t" \
  "v_exp_f32 v203, v203\n\t" \
  "v_smfmac_f32_32x32x32_bf16 v[32:47], v[240:243], v[192:199], v250\n\t" \
  "v_mov_b32 v224, 161024\n\t" \
  "ds_read_b32 v225, v224 offset:4\n\t" \
  "s_mul_i32 %[t], %[q0], 7\n\t" \
  "v_add_u32 v160, %[t], v248\n\t" \
  "s_mul_i32 %[t], %[q1], 7\n\t" \
  "v_add_u32 v161, %[t], v248\n\t" \
  "s_mul_i32 %[t], %[q2], 7\n\t" \
  "v_add_u32 v162, %[t], v248\n\t" \
  "s_mul_i32 %[t], %[q3], 7\n\t" \
  "v_add_u32 v163, %[t], v248\n\t" \
  "v_exp_f32 v204, v204\n\t" \
  "v_exp_f32 v205, v205\n\t" \
  "v_exp_f32 v206, v206\n\t" \
  "v_exp_f32 v207, v207\n\t" \
  "v_exp_f32 v208, v208\n\t" \
  "v_exp_f32 v209, v209\n\t" \
  "v_smfmac_f32_32x32x32_bf16 v[32:47], v[244:247], v[200:207], v250\n\t" \
  "v_exp_f32 v210, v210\n\t" \
  "v_exp_f32 v211, v211\n\t" \
  "v_exp_f32 v212, v212\n\t" \
  "v_exp_f32 v213, v213\n\t" \
  "v_exp_f32 v214, v214\n\t" \
  "v_mfma_f32_32x32x16_bf16 v[192:207], v[228:231], v[168:171], 0\n\t" \
  "v_exp_f32 v215, v215\n\t" \
  "v_exp_f32 v216, v216\n\t" \
  "v_exp_f32 v217, v217\n\t" \
  "v_exp_f32 v218, v218\n\t" \
  "v_exp_f32 v219, v219\n\t" \
  "v_smfmac_f32_32x32x32_bf16 v[48:63], v[240:243], v[208:215], v250\n\t" \
  "v_exp_f32 v220, v220\n\t" \
  "2:\n\t" \
  "s_waitcnt lgkmcnt(0)\n\t" \
  "v_readfirstlane_b32 %[cnt], v225\n\t" \
  "ds_read_b32 v225, v224 offset:4\n\t" \
  "s_cmp_lt_u32 %[cnt], 20\n\t" \
  "s_cbranch_scc1 2b\n\t" \
  "v_exp_f32 v221, v221\n\t" \
  "ds_write_b128 v160, v[32:35] offset:0\n\t" \
  "v_exp_f32 v222, v222\n\t" \
  "ds_write_b128 v160, v[36:39] offset:1024\n\t" \
  "v_exp_f32 v223, v223\n\t" \
  "ds_write_b32 v160, v40 offset:6144\n\t" \
  "v_exp_f32 v192, v192\n\t" \
  "v_exp_f32 v193, v193\n\t" \
  "v_smfmac_f32_32x32x32_bf16 v[48:63], v[244:247], v[216:223], v250\n\t" \
  "v_exp_f32 v194, v194\n\t" \
  "v_exp_f32 v195, v195\n\t" \
  "v_exp_f32 v196, v196\n\t" \
  "v_exp_f32 v197, v197\n\t" \
  "v_exp_f32 v198, v198\n\t" \
  "v_mfma_f32_32x32x16_bf16 v[208:223], v[228:231], v[172:175], 0\n\t" \
  "v_exp_f32 v199, v199\n\t" \
  "v_exp_f32 v200, v200\n\t" \
  "v_exp_f32 v201, v201\n\t" \
  "v_exp_f32 v202, v202\n\t" \
  "v_exp_f32 v203, v203\n\t" \
  "v_smfmac_f32_32x32x32_bf16 v[64:79], v[240:243], v[192:199], v250\n\t" \
  "v_exp_f32 v204, v204\n\t" \
  "v_exp_f32 v205, v205\n\t" \
  "ds_write_b128 v160, v[48:51] offset:7168\n\t" \
  "v_exp_f32 v206, v206\n\t" \
  "ds_write_b128 v160, v[52:55] offset:8192\n\t" \
  "v_exp_f32 v207, v207\n\t" \
  "ds_write_b32 v160, v56 offset:13312\n\t" \
  "v_exp_f32 v208, v208\n\t" \
  "v_exp_f32 v209, v209\n\t" \
  "v_smfmac_f32_32x32x32_bf16 v[64:79], v[244:247], v[200:207], v250\n\t" \
  "v_exp_f32 v210, v210\n\t" \
  "v_exp_f32 v211, v211\n\t" \
  "v_exp_f32 v212, v212\n\t" \
  "v_exp_f32 v213, v213\n\t" \
  "v_exp_f32 v214, v214\n\t" \
  "v_mfma_f32_32x32x16_bf16 v[192:207], v[228:231], v[176:179], 0\n\t" \
  "v_exp_f32 v215, v215\n\t" \
  "v_exp_f32 v216, v216\n\t" \
  "v_exp_f32 v217, v217\n\t" \
  "v_exp_f32 v218, v218\n\t" \
  "v_exp_f32 v219, v219\n\t" \
  "v_smfmac_f32_32x32x32_bf16 v[80:95], v[240:243], v[208:215], v250\n\t" \
  "v_exp_f32 v220, v220\n\t" \
  "v_exp_f32 v221, v221\n\t" \
  "ds_write_b128 v161, v[64:67] offset:2048\n\t" \
  "v_exp_f32 v222, v222\n\t" \
  "ds_write_b128 v161, v[68:71] offset:3072\n\t" \
  "v_exp_f32 v223, v223\n\t" \
  "ds_write_b32 v161, v72 offset:6148\n\t" \
  "v_exp_f32 v192, v192\n\t" \
  "v_exp_f32 v193, v193\n\t" \
  "v_smfmac_f32_32x32x32_bf16 v[80:95], v[244:247], v[216:223], v250\n\t" \
  "v_exp_f32 v194, v194\n\t" \
  "v_exp_f32 v195, v195\n\t" \
  "v_exp_f32 v196, v196\n\t" \
  "v_exp_f32 v197, v197\n\t" \
  "v_exp_f32 v198, v198\n\t" \
  "v_mfma_f32_32x32x16_bf16 v[208:223], v[228:231], v[180:183], 0\n\t" \
  "v_exp_f32 v199, v199\n\t" \
  "v_exp_f32 v200, v200\n\t" \
  "v_exp_f32 v201, v201\n\t" \
  "v_exp_f32 v202, v202\n\t" \
  "v_exp_f32 v203, v203\n\t" \
  "v_smfmac_f32_32x32x32_bf16 v[96:111], v[240:243], v[192:199], v250\n\t" \
  "v_exp_f32 v204, v204\n\t" \
  "v_exp_f32 v205, v205\n\t" \
  "ds_write_b128 v161, v[80:83] offset:9216\n\t" \
  "v_exp_f32 v206, v206\n\t" \
  "ds_write_b128 v161, v[84:87] offset:10240\n\t" \
  "v_exp_f32 v207, v207\n\t" \
  "ds_write_b32 v161, v88 offset:13316\n\t" \
  "v_exp_f32 v208, v208\n\t" \
  "v_exp_f32 v209, v209\n\t" \
  "v_smfmac_f32_32x32x32_bf16 v[96:111], v[244:247], v[200:207], v250\n\t" \
  "v_exp_f32 v210, v210\n\t" \
  "v_exp_f32 v211, v211\n\t" \
  "v_exp_f32 v212, v212\n\t" \
  "v_exp_f32 v213, v213\n\t" \
  "v_exp_f32 v214, v214\n\t" \
  "v_mfma_f32_32x32x16_bf16 v[192:207], v[228:231], v[184:187], 0\n\t" \
  "v_exp_f32 v215, v215\n\t" \
  "v_exp_f32 v216, v216\n\t" \
  "v_exp_f32 v217, v217\n\t" \
  "v_exp_f32 v218, v218\n\t" \
  "v_exp_f32 v219, v219\n\t" \
  "v_smfmac_f32_32x32x32_bf16 v[112:127], v[240:243], v[208:215], v250\n\t" \
  "v_exp_f32 v220, v220\n\t" \
  "v_exp_f32 v221, v221\n\t" \
  "ds_write_b128 v162, v[96:99] offset:4096\n\t" \
  "v_exp_f32 v222, v222\n\t" \
  "ds_write_b128 v162, v[100:103] offset:5120\n\t" \
  "v_exp_f32 v223, v223\n\t" \
  "ds_write_b32 v162, v104 offset:6152\n\t" \
  "v_exp_f32 v192, v192\n\t" \
  "v_exp_f32 v193, v193\n\t" \
  "v_smfmac_f32_32x32x32_bf16 v[112:127], v[244:247], v[216:223], v250\n\t" \
  "v_exp_f32 v194, v194\n\t" \
  "v_exp_f32 v195, v195\n\t" \
  "v_exp_f32 v196, v196\n\t" \
  "v_exp_f32 v197, v197\n\t" \
  "v_exp_f32 v198, v198\n\t" \
  "v_mfma_f32_32x32x16_bf16 v[208:223], v[228:231], v[188:191], 0\n\t" \
  "v_exp_f32 v199, v199\n\t" \
  "v_exp_f32 v200, v200\n\t" \
  "v_exp_f32 v201, v201\n\t" \
  "v_exp_f32 v202, v202\n\t" \
  "v_exp_f32 v203, v203\n\t" \
  "v_smfmac_f32_32x32x32_bf16 v[128:143], v[240:243], v[192:199], v250\n\t" \
  "v_exp_f32 v204, v204\n\t" \
  "v_exp_f32 v205, v205\n\t" \
  "ds_write_b128 v162, v[112:115] offset:11264\n\t" \
  "v_exp_f32 v206, v206\n\t" \
  "ds_write_b128 v162, v[116:119] offset:12288\n\t" \
  "v_exp_f32 v207, v207\n\t" \
  "ds_write_b32 v162, v120 offset:13320\n\t" \
  "v_mov_b32 v226, 1\n\t" \
  "s_mov_b64 exec, 1\n\t" \
  "ds_add_u32 v224, v226\n\t" \
  "s_mov_b64 exec, -1\n\t" \
  "v_exp_f32 v208, v208\n\t" \
  "v_exp_f32 v209, v209\n\t" \
  "v_smfmac_f32_32x32x32_bf16 v[128:143], v[244:247], v[200:207], v250\n\t" \
  "v_exp_f32 v210, v210\n\t" \
  "v_exp_f32 v211, v211\n\t" \
  "v_exp_f32 v212, v212\n\t" \
  "v_exp_f32 v213, v213\n\t" \
  "v_exp_f32 v214, v214\n\t" \
  "v_exp_f32 v215, v215\n\t" \
  "v_exp_f32 v216, v216\n\t" \
  "v_exp_f32 v217, v217\n\t" \
  "v_exp_f32 v218, v218\n\t" \
  "v_exp_f32 v219, v219\n\t" \
  "v_smfmac_f32_32x32x32_bf16 v[144:159], v[240:243], v[208:215], v250\n\t" \
  "ds_read_b32 v225, v224\n\t" \
  "v_bfe_u32 v226, v248, 2, 7\n\t" \
  "v_add_u32 v226, 160768, v226\n\t" \
  "ds_read2_b32 v[232:233], v226 offset1:32\n\t" \
  "v_exp_f32 v220, v220\n\t" \
  "v_exp_f32 v221, v221\n\t" \
  "v_exp_f32 v222, v222\n\t" \
  "v_exp_f32 v223, v223\n\t" \
  "s_nop 1\n\t" \
  "v_smfmac_f32_32x32x32_bf16 v[144:159], v[244:247], v[216:223], v250\n\t" \
  "s_mov_b32 %[t], 0x400000\n\t" \
  "8:\n\t" \
  "s_waitcnt lgkmcnt(0)\n\t" \
  "v_readfirstlane_b32 %[cnt], v225\n\t" \
  "s_cmp_ge_u32 %[cnt], 4\n\t" \
  "s_cbranch_scc1 9f\n\t" \
  "ds_read_b32 v225, v224\n\t" \
  "s_sub_u32 %[t], %[t], 1\n\t" \
  "s_cbranch_scc0 8b\n\t" \
  "9:\n\t" \
  "ds_read_b128 v[32:35], v163 offset:6144\n\t" \
  "ds_read_b128 v[36:39], v163 offset:0\n\t" \
  "ds_read_b128 v[40:43], v163 offset:2048\n\t" \
  "ds_read_b128 v[44:47], v163 offset:4096\n\t" \
  "ds_read_b128 v[48:51], v163 offset:1024\n\t" \
  "ds_read_b128 v[52:55], v163 offset:3072\n\t" \
  "ds_read_b128 v[56:59], v163 offset:5120\n\t" \
  "ds_read_b128 v[60:63], v163 offset:13312\n\t" \
  "ds_read_b128 v[64:67], v163 offset:7168\n\t" \
  "ds_read_b128 v[68:71], v163 offset:9216\n\t" \
  "ds_read_b128 v[72:75], v163 offset:11264\n\t" \
  "ds_read_b128 v[76:79], v163 offset:8192\n\t" \
  "ds_read_b128 v[80:83], v163 offset:10240\n\t" \
  "ds_read_b128 v[84:87], v163 offset:12288\n\t" \
  "s_mov_b64 %[ok], -1\n\t" \
  "s_waitcnt lgkmcnt(13)\n\t" \
  "v_add_f32 v32, v32, v33\n\t" \
  "v_add_f32 v136, v136, v34\n\t" \
  "v_add_f32 v136, v136, v32\n\t" \
  "v_rcp_f32 v192, v136\n\t" \
  "v_cmp_le_f32 vcc, 0x1f800000, v136\n\t" \
  "s_and_b64 %[ok], %[ok], vcc\n\t" \
  "v_cmp_ge_f32 vcc, 0x71800000, v136\n\t" \
  "s_and_b64 %[ok], %[ok], vcc\n\t" \
  "v_mul_f32 v192, v232, v192\n\t" \
  "s_waitcnt lgkmcnt(10)\n\t" \
  "v_add_f32 v128, v128, v36\n\t" \
  "v_add_f32 v129, v129, v37\n\t" \
  "v_add_f32 v130, v130, v38\n\t" \
  "v_add_f32 v131, v131, v39\n\t" \
  "v_add_f32 v40, v40, v44\n\t" \
  "v_add_f32 v41, v41, v45\n\t" \
  "v_add_f32 v42, v42, v46\n\t" \
  "v_add_f32 v43, v43, v47\n\t" \
  "v_add_f32 v128, v128, v40\n\t" \
  "v_add_f32 v129, v129, v41\n\t" \
  "v_add_f32 v130, v130, v42\n\t" \
  "v_add_f32 v131, v131, v43\n\t" \
  "s_waitcnt lgkmcnt(7)\n\t" \
  "v_add_f32 v132, v132, v48\n\t" \
  "v_add_f32 v133, v133, v49\n\t" \
  "v_add_f32 v134, v134, v50\n\t" \
  "v_add_f32 v135, v135, v51\n\t" \
  "v_add_f32 v52, v52, v56\n\t" \
  "v_add_f32 v53, v53, v57\n\t" \
  "v_add_f32 v54, v54, v58\n\t" \
  "v_add_f32 v55, v55, v59\n\t" \
  "v_add_f32 v132, v132, v52\n\t" \
  "v_add_f32 v133, v133, v53\n\t" \
  "v_add_f32 v134, v134, v54\n\t" \
  "v_add_f32 v135, v135, v55\n\t" \
  "v_add_f32 v32, |v128|, |v129|\n\t" \
  "v_add_f32 v33, |v130|, |v131|\n\t" \
  "v_add_f32 v34, |v132|, |v133|\n\t" \
  "v_add_f32 v35, |v134|, |v135|\n\t" \
  "v_add_f32 v32, v32, v33\n\t" \
  "v_add_f32 v34, v34, v35\n\t" \
  "v_add_f32 v193, v32, v34\n\t" \
  "v_cmp_ge_f32 vcc, 0x7e800000, v193\n\t" \
  "s_and_b64 %[ok], %[ok], vcc\n\t" \
  "v_mul_f32 v128, v192, v128\n\t" \
  "v_mul_f32 v129, v192, v129\n\t" \
  "v_mul_f32 v130, v192, v130\n\t" \
  "v_mul_f32 v131, v192, v131\n\t" \
  "v_mul_f32 v132, v192, v132\n\t" \
  "v_mul_f32 v133, v192, v133\n\t" \
  "v_mul_f32 v134, v192, v134\n\t" \
  "v_mul_f32 v135, v192, v135\n\t" \
  "v_cvt_pk_bf16_f32 v196, v128, v129\n\t" \
  "v_cvt_pk_bf16_f32 v197, v130, v131\n\t" \
  "v_cvt_pk_bf16_f32 v198, v132, v133\n\t" \
  "v_cvt_pk_bf16_f32 v199, v134, v135\n\t" \
  "v_min_u32 v196, -2, v196\n\t" \
  "v_min_u32 v197, -2, v197\n\t" \
  "v_min_u32 v198, -2, v198\n\t" \
  "v_min_u32 v199, -2, v199\n\t" \
  "s_waitcnt lgkmcnt(6)\n\t" \
  "v_add_f32 v60, v60, v61\n\t" \
  "v_add_f32 v152, v152, v62\n\t" \
  "v_add_f32 v152, v152, v60\n\t" \
  "v_rcp_f32 v192, v152\n\t" \
  "v_cmp_le_f32 vcc, 0x1f800000, v152\n\t" \
  "s_and_b64 %[ok], %[ok], vcc\n\t" \
  "v_cmp_ge_f32 vcc, 0x71800000, v152\n\t" \
  "s_and_b64 %[ok], %[ok], vcc\n\t" \
  "v_mul_f32 v192, v233, v192\n\t" \
  "s_waitcnt lgkmcnt(3)\n\t" \
  "v_add_f32 v144, v144, v64\n\t" \
  "v_add_f32 v145, v145, v65\n\t" \
  "v_add_f32 v146, v146, v66\n\t" \
  "v_add_f32 v147, v147, v67\n\t" \
  "v_add_f32 v68, v68, v72\n\t" \
  "v_add_f32 v69, v69, v73\n\t" \
  "v_add_f32 v70, v70, v74\n\t" \
  "v_add_f32 v71, v71, v75\n\t" \
  "v_add_f32 v144, v144, v68\n\t" \
  "v_add_f32 v145, v145, v69\n\t" \
  "v_add_f32 v146, v146, v70\n\t" \
  "v_add_f32 v147, v147, v71\n\t" \
  "s_waitcnt lgkmcnt(0)\n\t" \
  "v_add_f32 v148, v148, v76\n\t" \
  "v_add_f32 v149, v149, v77\n\t" \
  "v_add_f32 v150, v150, v78\n\t" \
  "v_add_f32 v151, v151, v79\n\t" \
  "v_add_f32 v80, v80, v84\n\t" \
  "v_add_f32 v81, v81, v85\n\t" \
  "v_add_f32 v82, v82, v86\n\t" \
  "v_add_f32 v83, v83, v87\n\t" \
  "v_add_f32 v148, v148, v80\n\t" \
  "v_add_f32 v149, v149, v81\n\t" \
  "v_add_f32 v150, v150, v82\n\t" \
  "v_add_f32 v151, v151, v83\n\t" \
  "v_add_f32 v60, |v144|, |v145|\n\t" \
  "v_add_f32 v61, |v146|, |v147|\n\t" \
  "v_add_f32 v62, |v148|, |v149|\n\t" \
  "v_add_f32 v63, |v150|, |v151|\n\t" \
  "v_add_f32 v60, v60, v61\n\t" \
  "v_add_f32 v62, v62, v63\n\t" \
  "v_add_f32 v193, v60, v62\n\t" \
  "v_cmp_ge_f32 vcc, 0x7e800000, v193\n\t" \
  "s_and_b64 %[ok], %[ok], vcc\n\t" \
  "v_mul_f32 v144, v192, v144\n\t" \
  "v_mul_f32 v145, v192, v145\n\t" \
  "v_mul_f32 v146, v192, v146\n\t" \
  "v_mul_f32 v147, v192, v147\n\t" \
  "v_mul_f32 v148, v192, v148\n\t" \
  "v_mul_f32 v149, v192, v149\n\t" \
  "v_mul_f32 v150, v192, v150\n\t" \
  "v_mul_f32 v151, v192, v151\n\t" \
  "v_cvt_pk_bf16_f32 v200, v144, v145\n\t" \
  "v_cvt_pk_bf16_f32 v201, v146, v147\n\t" \
  "v_cvt_pk_bf16_f32 v202, v148, v149\n\t" \
  "v_cvt_pk_bf16_f32 v203, v150, v151\n\t" \
  "v_min_u32 v200, -2, v200\n\t" \
  "v_min_u32 v201, -2, v201\n\t" \
  "v_min_u32 v202, -2, v202\n\t" \
  "v_min_u32 v203, -2, v203\n\t" \
  "s_cmp_eq_u64 %[ok], -1\n\t" \
  "s_cbranch_scc0 7f\n\t" \
  "buffer_store_dwordx4 v[196:199], v248, %[rs], %[od] offen sc1\n\t" \
  "s_add_u32 %[od], %[od], 147456\n\t" \
  "buffer_store_dwordx4 v[200:203], v248, %[rs], %[od] offen sc1\n\t" \
  "v_mov_b32 v226, 1\n\t" \
  "s_mov_b64 exec, 1\n\t" \
  "ds_add_u32 v224, v226 offset:8\n\t" \
  "s_mov_b64 exec, -1\n\t" \
  "7:\n\t" \
  "s_waitcnt lgkmcnt(0)\n\t" \
  ""
#define GEMM_TAIL_ASM \
  "buffer_load_dwordx4 v[144:147], %[lv], %[rs], %[a0] offen offset:0 sc1\n\t" \
  "buffer_load_dwordx4 v[148:151], %[lv], %[rs], %[b0] offen offset:0 sc1\n\t" \
  "buffer_load_dwordx4 v[152:155], %[lv], %[rs], %[a0] offen offset:1024 sc1\n\t" \
  "buffer_load_dwordx4 v[156:159], %[lv], %[rs], %[b0] offen offset:1024 sc1\n\t" \
  "buffer_load_dwordx4 v[160:163], %[lv], %[rs], %[a0] offen offset:2048 sc1\n\t" \
  "buffer_load_dwordx4 v[164:167], %[lv], %[rs], %[b0] offen offset:2048 sc1\n\t" \
  "buffer_load_dwordx4 v[168:171], %[lv], %[rs], %[a0] offen offset:3072 sc1\n\t" \
  "buffer_load_dwordx4 v[172:175], %[lv], %[rs], %[b0] offen offset:3072 sc1\n\t" \
  "buffer_load_dwordx4 v[176:179], %[lv], %[rs], %[a1] offen offset:0 sc1\n\t" \
  "buffer_load_dwordx4 v[180:183], %[lv], %[rs], %[b1] offen offset:0 sc1\n\t" \
  "buffer_load_dwordx4 v[184:187], %[lv], %[rs], %[a1] offen offset:1024 sc1\n\t" \
  "buffer_load_dwordx4 v[188:191], %[lv], %[rs], %[b1] offen offset:1024 sc1\n\t" \
  "buffer_load_dwordx4 v[192:195], %[lv], %[rs], %[a1] offen offset:2048 sc1\n\t" \
  "buffer_load_dwordx4 v[196:199], %[lv], %[rs], %[b1] offen offset:2048 sc1\n\t" \
  "buffer_load_dwordx4 v[200:203], %[lv], %[rs], %[a1] offen offset:3072 sc1\n\t" \
  "buffer_load_dwordx4 v[204:207], %[lv], %[rs], %[b1] offen offset:3072 sc1\n\t" \
  "s_waitcnt vmcnt(8)\n\t" \
  "v_max3_u32 v208, v144, v145, v146\n\t" \
  "v_max3_u32 v209, v147, v148, v149\n\t" \
  "v_max3_u32 v209, v150, v151, v209\n\t" \
  "v_max3_u32 v208, v152, v153, v208\n\t" \
  "v_max3_u32 v209, v154, v155, v209\n\t" \
  "v_max3_u32 v208, v156, v157, v208\n\t" \
  "v_max3_u32 v209, v158, v159, v209\n\t" \
  "v_max3_u32 v208, v160, v161, v208\n\t" \
  "v_max3_u32 v209, v162, v163, v209\n\t" \
  "v_max3_u32 v208, v164, v165, v208\n\t" \
  "v_max3_u32 v209, v166, v167, v209\n\t" \
  "v_max3_u32 v208, v168, v169, v208\n\t" \
  "v_max3_u32 v209, v170, v171, v209\n\t" \
  "v_max3_u32 v208, v172, v173, v208\n\t" \
  "v_max3_u32 v209, v174, v175, v209\n\t" \
  "v_max_u32 v208, v208, v209\n\t" \
  "v_cmp_eq_u32 vcc, -1, v208\n\t" \
  "s_cbranch_vccz 24f\n\t" \
  "20:\n\t" \
  "buffer_load_dwordx4 v[144:147], %[lv], %[rs], %[a0] offen offset:0 sc1\n\t" \
  "buffer_load_dwordx4 v[148:151], %[lv], %[rs], %[b0] offen offset:0 sc1\n\t" \
  "buffer_load_dwordx4 v[152:155], %[lv], %[rs], %[a0] offen offset:1024 sc1\n\t" \
  "buffer_load_dwordx4 v[156:159], %[lv], %[rs], %[b0] offen offset:1024 sc1\n\t" \
  "buffer_load_dwordx4 v[160:163], %[lv], %[rs], %[a0] offen offset:2048 sc1\n\t" \
  "buffer_load_dwordx4 v[164:167], %[lv], %[rs], %[b0] offen offset:2048 sc1\n\t" \
  "buffer_load_dwordx4 v[168:171], %[lv], %[rs], %[a0] offen offset:3072 sc1\n\t" \
  "buffer_load_dwordx4 v[172:175], %[lv], %[rs], %[b0] offen offset:3072 sc1\n\t" \
  "s_waitcnt vmcnt(0)\n\t" \
  "v_max3_u32 v208, v144, v145, v146\n\t" \
  "v_max3_u32 v209, v147, v148, v149\n\t" \
  "v_max3_u32 v209, v150, v151, v209\n\t" \
  "v_max3_u32 v208, v152, v153, v208\n\t" \
  "v_max3_u32 v209, v154, v155, v209\n\t" \
  "v_max3_u32 v208, v156, v157, v208\n\t" \
  "v_max3_u32 v209, v158, v159, v209\n\t" \
  "v_max3_u32 v208, v160, v161, v208\n\t" \
  "v_max3_u32 v209, v162, v163, v209\n\t" \
  "v_max3_u32 v208, v164, v165, v208\n\t" \
  "v_max3_u32 v209, v166, v167, v209\n\t" \
  "v_max3_u32 v208, v168, v169, v208\n\t" \
  "v_max3_u32 v209, v170, v171, v209\n\t" \
  "v_max3_u32 v208, v172, v173, v208\n\t" \
  "v_max3_u32 v209, v174, v175, v209\n\t" \
  "v_max_u32 v208, v208, v209\n\t" \
  "v_cmp_eq_u32 vcc, -1, v208\n\t" \
  "s_cbranch_vccz 21f\n\t" \
  "s_sub_u32 %[cap], %[cap], 1\n\t" \
  "s_cbranch_scc0 20b\n\t" \
  "21:\n\t" \
  "buffer_load_dwordx4 v[176:179], %[lv], %[rs], %[a1] offen offset:0 sc1\n\t" \
  "buffer_load_dwordx4 v[180:183], %[lv], %[rs], %[b1] offen offset:0 sc1\n\t" \
  "buffer_load_dwordx4 v[184:187], %[lv], %[rs], %[a1] offen offset:1024 sc1\n\t" \
  "buffer_load_dwordx4 v[188:191], %[lv], %[rs], %[b1] offen offset:1024 sc1\n\t" \
  "buffer_load_dwordx4 v[192:195], %[lv], %[rs], %[a1] offen offset:2048 sc1\n\t" \
  "buffer_load_dwordx4 v[196:199], %[lv], %[rs], %[b1] offen offset:2048 sc1\n\t" \
  "buffer_load_dwordx4 v[200:203], %[lv], %[rs], %[a1] offen offset:3072 sc1\n\t" \
  "buffer_load_dwordx4 v[204:207], %[lv], %[rs], %[b1] offen offset:3072 sc1\n\t" \
  "24:\n\t" \
  "v_mfma_f32_32x32x16_bf16 v[16:31], v[80:83], v[144:147], v[16:31]\n\t" \
  "v_mfma_f32_32x32x16_bf16 v[32:47], v[80:83], v[148:151], v[32:47]\n\t" \
  "v_mfma_f32_32x32x16_bf16 v[48:63], v[84:87], v[144:147], v[48:63]\n\t" \
  "v_mfma_f32_32x32x16_bf16 v[64:79], v[84:87], v[148:151], v[64:79]\n\t" \
  "v_mfma_f32_32x32x16_bf16 v[16:31], v[88:91], v[152:155], v[16:31]\n\t" \
  "v_mfma_f32_32x32x16_bf16 v[32:47], v[88:91], v[156:159], v[32:47]\n\t" \
  "v_mfma_f32_32x32x16_bf16 v[48:63], v[92:95], v[152:155], v[48:63]\n\t" \
  "s_nop 0\n\t" \
  "s_nop 0\n\t" \
  "s_nop 0\n\t" \
  "v_mfma_f32_32x32x16_bf16 v[64:79], v[92:95], v[156:159], v[64:79]\n\t" \
  "s_nop 0\n\t" \
  "s_nop 0\n\t" \
  "s_nop 0\n\t" \
  "v_mfma_f32_32x32x16_bf16 v[16:31], v[96:99], v[160:163], v[16:31]\n\t" \
  "s_nop 0\n\t" \
  "s_nop 0\n\t" \
  "s_nop 0\n\t" \
  "v_mfma_f32_32x32x16_bf16 v[32:47], v[96:99], v[164:167], v[32:47]\n\t" \
  "s_waitcnt vmcnt(0)\n\t" \
  "v_max3_u32 v208, v176, v177, v178\n\t" \
  "v_max3_u32 v209, v179, v180, v181\n\t" \
  "v_mfma_f32_32x32x16_bf16 v[48:63], v[100:103], v[160:163], v[48:63]\n\t" \
  "v_max3_u32 v209, v182, v183, v209\n\t" \
  "v_max3_u32 v208, v184, v185, v208\n\t" \
  "v_max3_u32 v209, v186, v187, v209\n\t" \
  "v_mfma_f32_32x32x16_bf16 v[64:79], v[100:103], v[164:167], v[64:79]\n\t" \
  "v_max3_u32 v208, v188, v189, v208\n\t" \
  "v_max3_u32 v209, v190, v191, v209\n\t" \
  "v_max3_u32 v208, v192, v193, v208\n\t" \
  "v_mfma_f32_32x32x16_bf16 v[16:31], v[104:107], v[168:171], v[16:31]\n\t" \
  "v_max3_u32 v209, v194, v195, v209\n\t" \
  "v_max3_u32 v208, v196, v197, v208\n\t" \
  "v_max3_u32 v209, v198, v199, v209\n\t" \
  "v_mfma_f32_32x32x16_bf16 v[32:47], v[104:107], v[172:175], v[32:47]\n\t" \
  "v_max3_u32 v208, v200, v201, v208\n\t" \
  "v_max3_u32 v209, v202, v203, v209\n\t" \
  "v_max3_u32 v208, v204, v205, v208\n\t" \
  "v_mfma_f32_32x32x16_bf16 v[48:63], v[108:111], v[168:171], v[48:63]\n\t" \
  "v_max3_u32 v209, v206, v207, v209\n\t" \
  "v_max_u32 v208, v208, v209\n\t" \
  "v_cmp_eq_u32 vcc, -1, v208\n\t" \
  "v_mfma_f32_32x32x16_bf16 v[64:79], v[108:111], v[172:175], v[64:79]\n\t" \
  "s_cbranch_vccz 23f\n\t" \
  "22:\n\t" \
  "buffer_load_dwordx4 v[176:179], %[lv], %[rs], %[a1] offen offset:0 sc1\n\t" \
  "buffer_load_dwordx4 v[180:183], %[lv], %[rs], %[b1] offen offset:0 sc1\n\t" \
  "buffer_load_dwordx4 v[184:187], %[lv], %[rs], %[a1] offen offset:1024 sc1\n\t" \
  "buffer_load_dwordx4 v[188:191], %[lv], %[rs], %[b1] offen offset:1024 sc1\n\t" \
  "buffer_load_dwordx4 v[192:195], %[lv], %[rs], %[a1] offen offset:2048 sc1\n\t" \
  "buffer_load_dwordx4 v[196:199], %[lv], %[rs], %[b1] offen offset:2048 sc1\n\t" \
  "buffer_load_dwordx4 v[200:203], %[lv], %[rs], %[a1] offen offset:3072 sc1\n\t" \
  "buffer_load_dwordx4 v[204:207], %[lv], %[rs], %[b1] offen offset:3072 sc1\n\t" \
  "s_waitcnt vmcnt(0)\n\t" \
  "v_max3_u32 v208, v176, v177, v178\n\t" \
  "v_max3_u32 v209, v179, v180, v181\n\t" \
  "v_max3_u32 v209, v182, v183, v209\n\t" \
  "v_max3_u32 v208, v184, v185, v208\n\t" \
  "v_max3_u32 v209, v186, v187, v209\n\t" \
  "v_max3_u32 v208, v188, v189, v208\n\t" \
  "v_max3_u32 v209, v190, v191, v209\n\t" \
  "v_max3_u32 v208, v192, v193, v208\n\t" \
  "v_max3_u32 v209, v194, v195, v209\n\t" \
  "v_max3_u32 v208, v196, v197, v208\n\t" \
  "v_max3_u32 v209, v198, v199, v209\n\t" \
  "v_max3_u32 v208, v200, v201, v208\n\t" \
  "v_max3_u32 v209, v202, v203, v209\n\t" \
  "v_max3_u32 v208, v204, v205, v208\n\t" \
  "v_max3_u32 v209, v206, v207, v209\n\t" \
  "v_max_u32 v208, v208, v209\n\t" \
  "v_cmp_eq_u32 vcc, -1, v208\n\t" \
  "s_cbranch_vccz 23f\n\t" \
  "s_sub_u32 %[cap], %[cap], 1\n\t" \
  "s_cbranch_scc0 22b\n\t" \
  "23:\n\t" \
  "v_mfma_f32_32x32x16_bf16 v[32:47], v[112:115], v[180:183], v[32:47]\n\t" \
  "v_mfma_f32_32x32x16_bf16 v[32:47], v[120:123], v[188:191], v[32:47]\n\t" \
  "v_mfma_f32_32x32x16_bf16 v[32:47], v[128:131], v[196:199], v[32:47]\n\t" \
  "v_mfma_f32_32x32x16_bf16 v[32:47], v[136:139], v[204:207], v[32:47]\n\t" \
  "v_mfma_f32_32x32x16_bf16 v[48:63], v[116:119], v[176:179], v[48:63]\n\t" \
  "v_mfma_f32_32x32x16_bf16 v[48:63], v[124:127], v[184:187], v[48:63]\n\t" \
  "v_mfma_f32_32x32x16_bf16 v[48:63], v[132:135], v[192:195], v[48:63]\n\t" \
  "ds_write_b128 %[sp], v[32:35] offset:0\n\t" \
  "ds_write_b128 %[sp], v[36:39] offset:1024\n\t" \
  "v_mfma_f32_32x32x16_bf16 v[48:63], v[140:143], v[200:203], v[48:63]\n\t" \
  "ds_write_b128 %[sp], v[40:43] offset:2048\n\t" \
  "ds_write_b128 %[sp], v[44:47] offset:3072\n\t" \
  "v_mfma_f32_32x32x16_bf16 v[64:79], v[116:119], v[180:183], v[64:79]\n\t" \
  "v_mfma_f32_32x32x16_bf16 v[64:79], v[124:127], v[188:191], v[64:79]\n\t" \
  "v_mfma_f32_32x32x16_bf16 v[64:79], v[132:135], v[196:199], v[64:79]\n\t" \
  "ds_write_b128 %[sp], v[48:51] offset:4096\n\t" \
  "ds_write_b128 %[sp], v[52:55] offset:5120\n\t" \
  "v_mfma_f32_32x32x16_bf16 v[64:79], v[140:143], v[204:207], v[64:79]\n\t" \
  "ds_write_b128 %[sp], v[56:59] offset:6144\n\t" \
  "ds_write_b128 %[sp], v[60:63] offset:7168\n\t" \
  "v_mfma_f32_32x32x16_bf16 v[16:31], v[112:115], v[176:179], v[16:31]\n\t" \
  "v_mfma_f32_32x32x16_bf16 v[16:31], v[120:123], v[184:187], v[16:31]\n\t" \
  "v_mfma_f32_32x32x16_bf16 v[16:31], v[128:131], v[192:195], v[16:31]\n\t" \
  "ds_write_b128 %[sp], v[64:67] offset:8192\n\t" \
  "ds_write_b128 %[sp], v[68:71] offset:9216\n\t" \
  "v_mfma_f32_32x32x16_bf16 v[16:31], v[136:139], v[200:203], v[16:31]\n\t" \
  "ds_write_b128 %[sp], v[72:75] offset:10240\n\t" \
  "ds_write_b128 %[sp], v[76:79] offset:11264\n\t" \
  "40:\n\t" \
  "v_mov_b32 v208, 161024\n\t" \
  "v_mov_b32 v209, 1\n\t" \
  "s_mov_b64 exec, 1\n\t" \
  "ds_add_u32 v208, v209 offset:12\n\t" \
  "s_mov_b64 exec, -1\n\t" \
  "41:\n\t" \
  "ds_read_b32 v209, v208 offset:12\n\t" \
  "s_waitcnt lgkmcnt(0)\n\t" \
  "v_readfirstlane_b32 %[t], v209\n\t" \
  "s_cmp_ge_u32 %[t], 4\n\t" \
  "s_cbranch_scc1 42f\n\t" \
  "s_sub_u32 %[cap], %[cap], 1\n\t" \
  "s_cbranch_scc0 41b\n\t" \
  "42:\n\t" \
  "s_nop 3\n\t" \
  ""
#define GT_CLOBBER "v144", "v145", "v146", "v147", "v148", "v149", "v150", "v151", "v152", "v153", "v154", "v155", "v156", "v157", "v158", "v159", "v160", "v161", "v162", "v163", "v164", "v165", "v166", "v167", "v168", "v169", "v170", "v171", "v172", "v173", "v174", "v175", "v176", "v177", "v178", "v179", "v180", "v181", "v182", "v183", "v184", "v185", "v186", "v187", "v188", "v189", "v190", "v191", "v192", "v193", "v194", "v195", "v196", "v197", "v198", "v199", "v200", "v201", "v202", "v203", "v204", "v205", "v206", "v207", "v208", "v209"

__global__ __launch_bounds__(512, 2) void fused_block(const u16* __restrict__ x, const u16* __restrict__ wq, const u16* __restrict__ wk,
                                                      const u16* __restrict__ wv, const u16* __restrict__ wo, const u16* __restrict__ w1,
                                                      const u16* __restrict__ w2, const u16* __restrict__ gatew, char* ws, u16* __restrict__ out) {
  extern __shared__ __attribute__((aligned(16))) char smem[];
  LAS char* lds = (LAS char*)smem;
  const unsigned lds0 = (unsigned)(uintptr_t)smem;
  const int b = blockIdx.x, t = b >> 3, bi = (b & 7) + 8 * (t >> 3), bj = t & 7;
  const int lane = threadIdx.x & 63, w = __builtin_amdgcn_readfirstlane(threadIdx.x >> 6);
  const int r = lane & 31, h = lane >> 5, l16 = lane & 15, g4 = lane >> 4;
  const int trq = l16 >> 2, trp = lane & 3, trcb = g4 & 1;
  const rsrc_t rs = __builtin_amdgcn_make_buffer_rsrc(ws, 0, WS_BYTES, 0x00020000);
  const u32x4 rsv = {(unsigned)(uintptr_t)ws, (unsigned)((uintptr_t)ws >> 32) & 0xffffu, (unsigned)WS_BYTES, 0x00020000u};
  LAS unsigned* cnt = (LAS unsigned*)(lds + L_CNT);
  const char* xb = (const char*)(x + (size_t)bi * 64 * D);
  const unsigned wv1k = (unsigned)((r >> 2) * 1024 + h * 64 + (lane & 3) * 16);
  const unsigned lv = lane * 16;
  unsigned long long pc; asm volatile("s_getpc_b64 %0" : "=s"(pc) : "s"(x), "s"(wq), "s"(wk), "s"(wv), "s"(wo), "s"(w1), "s"(w2), "s"(gatew), "s"(ws), "s"(out));
#define SELMASK(c) (0ull - (unsigned long long)(c))
  const int xg = bj >> 1, qg = bi >> 3;
  {
    const unsigned long long wkv = (((uintptr_t)wv & SELMASK(bj & 1)) | ((uintptr_t)wk & SELMASK(!(bj & 1)))) + w * 2048;
    asm volatile("s_mov_b32 m0, %5\n\ts_nop 0\n\tglobal_load_lds_dwordx4 %0, %4\n\tglobal_load_lds_dwordx4 %1, %4 offset:1024\n\t"
                 "s_mov_b32 m0, %7\n\ts_nop 0\n\tglobal_load_lds_dwordx4 %2, %6\n\tglobal_load_lds_dwordx4 %2, %6 offset:1024\n\t"
                 "s_mov_b32 m0, %10\n\ts_barrier\n\tglobal_load_lds_dwordx4 %3, %8\n\ts_add_u32 m0, m0, 1024\n\ts_nop 0\n\tglobal_load_lds_dwordx4 %3, %9"
                 :: "v"(lv ^ (w * 32)), "v"(lv ^ (w * 32 + 16)), "v"(lv), "v"(wv1k), "s"(xb + (xg * 16 + w * 2) * 1024), "s"(lds0 + L_X + (xg * 16 + w * 2) * 1024),
                    "s"(wkv), "s"(lds0 + L_WKV + w * 2048), "s"((const char*)wq + bj * 128 + (qg * 16 + w * 2) * 8192),
                    "s"((const char*)wq + bj * 128 + (qg * 16 + w * 2 + 1) * 8192), "s"(lds0 + L_WQ + (qg * 16 + w * 2) * 1024) : "memory", "scc");
  }
  SBAR();
  const unsigned long long touch0 = (((uintptr_t)w1 + b * 7168) & SELMASK((w & 3) == 0)) | (((uintptr_t)w2 + b * 7168) & SELMASK((w & 3) == 1)) |
                                    (((uintptr_t)wo + b * 2048) & SELMASK((w & 3) == 2)) | ((uintptr_t)gatew & SELMASK((w & 3) == 3));
  const unsigned long long touch1 = (((uintptr_t)out + bi * 65536 + bj * 128) & SELMASK(w & 1)) | (((uintptr_t)ws + WS_AF + (bi * 2 * KS_B + KS_H + bj * 4) * 1024) & SELMASK(!(w & 1)));
  if (w < 4) {
    f32x32 o01, o23, o45, o67; u32x16 vv; u32x8 k0;
    const unsigned vconst = (r == 16 || r == 20) ? 0x3f803f80u : 0u;
    unsigned c2, c3;
    asm volatile("s_load_dword %0, %2, 0x0\n\ts_load_dword %1, %3, 0x0" : "=&s"(c2), "=&s"(c3) : "s"(touch0), "s"(touch1) : "memory");
    asm volatile(ATTN_INIT_ASM : "=&{v[32:63]}"(o01), "=&{v[64:95]}"(o23), "=&{v[96:127]}"(o45), "=&{v[128:159]}"(o67));
    asm volatile("s_waitcnt vmcnt(2)\n\ts_barrier" ::: "memory");
    if (w < 3) {
      const int g = (xg + 1 + w) & 3;
      unsigned vx, k = 0;
      asm volatile("s_mov_b32 m0, %4\n\t1:\n\tv_xor_b32 %0, %1, %2\n\tglobal_load_lds_dwordx4 %0, %3\n\ts_add_u32 m0, m0, 1024\n\ts_add_u32 %1, %1, 1040\n\ts_cmp_lg_u32 %1, 16640\n\ts_cbranch_scc1 1b"
                   : "=&v"(vx), "+s"(k) : "v"(lv), "s"(xb + g * 16384), "s"(lds0 + L_X + g * 16384) : "memory", "scc");
    } else {
      glds16(gatew, lv, lds0 + L_GW);
    }
    const unsigned long long vm = 0x0000FFFF0000FFFFull;
    const unsigned sb = WS_KV + w * 32768;
    unsigned so = sb + (t & 7) * 4096;
    unsigned tag;
    unsigned voff = h * 256 + (r & 15) * 16;
    {
      const LAS char* xq[8];
#pragma unroll
      for (int i = 0; i < 8; ++i) { xq[i] = lds + L_X + ((w & 1) * 32 + r) * 1024 + (((2 * i + h) ^ (r & 15)) * 16); asm volatile("" : "+v"(xq[i])); }
      const LAS char* wrd = lds + L_WQ + h * 1024 + (w >> 1) * 512 + trq * 64 + trcb * 32 + trp * 8;
      LAS char* qd = lds + L_QF + ((w >> 1) * 4 + (w & 1)) * 1024 + lane * 16;
      int fo = WS_FKV + (b & 7) * 1024 + w * 256 + lane * 4;
      wait_vm<0>();
      asm volatile("" : "+v"(wrd), "+v"(qd), "+v"(fo), "+v"(voff) :: "memory");
      BARRIER();
      asm volatile("" :: "s"(c2), "s"(c3));
      f32x16 acc = {};
      u32x4 wf[5], xf[5];
      unsigned f = 0, tg = 0;
#pragma unroll
      for (int s = 0; s < 36; ++s) {
        if (s < 32) { wf[s % 5] = tr_frag(wrd + s * 2048, 256); xf[s % 5] = lds128(xq[s & 7] + (s >> 3) * 256); }
        if (s == FPOLL) asm volatile("buffer_load_dword %0, %1, %2, 0 offen sc1" : "=v"(f) : "v"(fo), "s"(rsv) : "memory");
        if (s == 5) tg = cnt[7];
        SBAR();
        if (s >= 4) acc = mfma32(wf[(s - 4) % 5], xf[(s - 4) % 5], acc);
        if (s == FCHECK) {
          tag = __builtin_amdgcn_readfirstlane(tg);
          unsigned cap = SPIN_CAP;
          asm volatile(ATTN_FIRST_ASM : "=&{v[224:231]}"(k0), "=&{v[232:247]}"(vv), [f] "+v"(f), [cap] "+s"(cap)
                       : "{v248}"(lv), "{v249}"(voff), [rs] "s"(rsv), [so] "s"(so), [vm] "s"(vm), [vc] "v"(vconst), [fo] "v"(fo), [tag] "s"(tag) : "memory", "scc", "vcc");
        }
        SBAR();
      }
      *(LAS u32x4*)qd = pack8(acc, 0, QSCALE);
      *(LAS u32x4*)(qd + 2048) = pack8(acc, 8, QSCALE);
      BARRIER();
    }
    unsigned long long ok;
    {
      int c = 7; unsigned tmp, od = WS_AF + (bi * 2 * KS_B + KS_H + bj * 4 + w) * 1024;
      static_assert(L_X == 0 && L_QF == 147456 && L_G == 160768 && L_CNT == 161024, "LDS addresses inside the generated code");
      asm volatile(ATTN_MAIN_ASM
                   : "+{v[32:63]}"(o01), "+{v[64:95]}"(o23), "+{v[96:127]}"(o45), "+{v[128:159]}"(o67), "+{v[232:247]}"(vv), "+{v[224:231]}"(k0), [so] "+s"(so), [cnt] "+s"(c),
                     [od] "+s"(od), [t] "=&s"(tmp), [ok] "=&s"(ok)
                   : "{v248}"(lv), "{v249}"(voff), [rs] "s"(rsv), [vm] "s"(vm), [sb] "s"(sb),
                     [q0] "s"(((1 + w) & 3) * 2048), [q1] "s"(((2 + w) & 3) * 2048), [q2] "s"(((3 + w) & 3) * 2048), [q3] "s"(w * 2048)
                   : "memory", "scc", "vcc", "v250", "v160", "v161", "v162", "v163", "v164", "v165", "v166", "v167", "v168", "v169", "v170", "v171", "v172", "v173", "v174", "v175", "v176", "v177", "v178", "v179", "v180", "v181", "v182", "v183", "v184", "v185", "v186", "v187", "v188", "v189", "v190", "v191", "v192", "v193", "v194", "v195", "v196", "v197", "v198", "v199", "v200", "v201", "v202", "v203", "v204", "v205", "v206", "v207", "v208", "v209", "v210", "v211", "v212", "v213", "v214", "v215", "v216", "v217", "v218", "v219", "v220", "v221", "v222", "v223");
    }
    if (__builtin_expect(ok != ~0ull, 0)) {
#pragma unroll 1
      for (int rt = 0; rt < 2; ++rt) {
        const u32x4 qf = lds128(lds + L_QF + (2 * w + rt) * 1024 + lane * 16);
        float mx = -INFINITY;
        const f32x16 z = {};
#pragma unroll 1
        for (int kt = 0; kt < 64; ++kt) {
          const f32x16 s = mfma32(__builtin_amdgcn_raw_buffer_load_b128(rs, WS_KV + kt * 2048 + lane * 16, 0, SC1), qf, z);
#pragma unroll
          for (int i = 0; i < 16; ++i) mx = fmaxf(mx, s[i]);
        }
        mx = fmaxf(mx, __shfl_xor(mx, 32));
        f32x16 nm, o = {};
#pragma unroll
        for (int i = 0; i < 16; ++i) nm[i] = -mx;
        const u32x4 vc4 = {vconst, vconst, vconst, vconst};
#pragma unroll 1
        for (int kt = 0; kt < 64; ++kt) {
          f32x16 s = mfma32(__builtin_amdgcn_raw_buffer_load_b128(rs, WS_KV + kt * 2048 + lane * 16, 0, SC1), qf, nm);
          u32x4 v0 = __builtin_amdgcn_raw_buffer_load_b128(rs, WS_KV + kt * 2048 + 1024 + h * 256 + (r & 15) * 16, 0, SC1);
          u32x4 v1 = __builtin_amdgcn_raw_buffer_load_b128(rs, WS_KV + kt * 2048 + 1536 + h * 256 + (r & 15) * 16, 0, SC1);
          if (r >= 16) { v0 = vc4; v1 = vc4; }
#pragma unroll
          for (int i = 0; i < 16; ++i) s[i] = __builtin_amdgcn_exp2f(s[i]);
          o = smfmac_hi(v0, lo8(s), o, 0xdddd);
          o = smfmac_hi(v1, hi8(s), o, 0xdddd);
        }
        const float sc = ((const LAS float*)(lds + L_G))[rt * 32 + r] * __builtin_amdgcn_rcpf(o[8]);
        u32x4 of = pack8(o, 0, sc);
#pragma unroll
        for (int i = 0; i < 4; ++i) of[i] = min(of[i], EMPTY - 1u);
        __builtin_amdgcn_raw_buffer_store_b128(of, rs, WS_AF + ((bi * 2 + rt) * KS_B + KS_H + bj * 4 + w) * 1024 + lane * 16, 0, SC1);
      }
      if (lane == 0) __hip_atomic_fetch_add(cnt + 2, 1u, __ATOMIC_RELAXED, __HIP_MEMORY_SCOPE_WORKGROUP);
    }
    if (b == 0 && w == 0 && lane == 0) __builtin_amdgcn_raw_buffer_store_b32(tag, rs, WS_EP, 0, SC1);
  } else {
    const int gw = w - 4;
    __builtin_amdgcn_s_setprio(3);
    {
      unsigned e, c0, c1;
      asm volatile("s_cmp_eq_u32 %5, 0\n\ts_cbranch_scc1 7f\n\ts_load_dword %0, %3, 0x0\n\ts_load_dword %1, %4, 0x0\n\ts_load_dword %2, %4, 0x80\n\t7:" : "=&s"(e), "=&s"(c0), "=&s"(c1)
                   : "s"(ws + WS_EP), "s"((pc & ~255ull) + t * (CODE_TOUCH / 32) + (gw - 1) * 256), "s"(gw) : "memory", "scc");
      const int kt = bi * 2 + (bj >> 2), sub = (bj >> 1) & 1;
      const int offv = 1024 + sub * 512 + (g4 >> 1) * 256 + l16 * 16 + (g4 & 1) * 8, offk = ((g4 & 1) * 32 + sub * 16 + l16) * 16 + (g4 >> 1) * 8;
      int off = WS_KV + kt * 2048 + ((bj & 1) ? offv : offk);
      const LAS char* wr = lds + L_WKV + (8 * g4 + trq) * 32 + trp * 8;
      const LAS char* xr[4];
#pragma unroll
      for (int i = 0; i < 4; ++i) { xr[i] = lds + L_X + (xg * 16 + l16) * 1024 + (((4 * i + g4) ^ l16) * 16); asm volatile("" : "+v"(xr[i])); }
      asm volatile("s_waitcnt vmcnt(2)\n\ts_barrier" : "+v"(off), "+v"(wr) :: "memory");
      if (gw == 0) {
        u32x4 xf[16], wf[16];
#pragma unroll
        for (int s = 0; s < 16; ++s) { xf[s] = lds128(xr[s & 3] + (s >> 2) * 256); wf[s] = tr_frag(wr + s * 1024, 128); }
        SBAR();
        f32x4 a0 = {}, a1 = {};
        if (bj & 1) {
#pragma unroll
          for (int s = 0; s < 16; s += 2) { a0 = mfma16(xf[s], wf[s], a0); a1 = mfma16(xf[s + 1], wf[s + 1], a1); }
        } else {
#pragma unroll
          for (int s = 0; s < 16; s += 2) { a0 = mfma16(wf[s], xf[s], a0); a1 = mfma16(wf[s + 1], xf[s + 1], a1); }
        }
        a0 += a1;
        const u32x2 v = {pk2(a0[0], a0[1]), pk2(a0[2], a0[3])};
        __builtin_amdgcn_raw_buffer_store_b64(v, rs, off, 0, SC1);
        unsigned ep;
        asm volatile("s_load_dword %0, %1, 0x0\n\ts_waitcnt lgkmcnt(0)" : "=s"(ep) : "s"(ws + WS_EP) : "memory");
        const unsigned tg = ep * 0x9E3779B1u + 0x7F4A7C15u;
        if (lane < 8) cnt[lane] = lane == 7 ? tg : 0u;
        wait_vm<0>();
        if (lane < 8) __builtin_amdgcn_raw_buffer_store_b32(tg, rs, WS_FKV + lane * 1024 + b * 4, 0, SC1);
      } else {
        __builtin_amdgcn_s_setprio(0);
        const int g = (qg + gw) & 3;
        const char* wqb = (const char*)wq + bj * 128 + (size_t)g * 16 * 8192;
        unsigned vq = wv1k, k = 16;
        asm volatile("s_mov_b32 m0, %3\n\ts_nop 0\n\t1:\n\tglobal_load_lds_dwordx4 %0, %2\n\ts_add_u32 m0, m0, 1024\n\tv_add_u32 %0, 0x2000, %0\n\ts_sub_u32 %1, %1, 1\n\ts_cmp_lg_u32 %1, 0\n\ts_cbranch_scc1 1b"
                     : "+v"(vq), "+s"(k) : "s"(wqb), "s"(lds0 + L_WQ + g * 16384) : "memory", "scc");
      }
      asm volatile("s_waitcnt lgkmcnt(0)" : "+s"(e), "+s"(c0), "+s"(c1)::"memory");
    }
    __builtin_amdgcn_s_setprio(0);
    unsigned cw;
    TOUCH14(cw, (const char*)w1 + b * 7168 + gw * 1792);
    if (gw != 0) wait_vm<0>();
    asm volatile("s_barrier" ::: "memory");
    const unsigned tag = __builtin_amdgcn_readfirstlane(cnt[7]);
    const unsigned ring = lds0 + L_WQ + gw * 16384;
    const LAS char* ringp = lds + L_WQ + gw * 16384;
    const char* w1b = (const char*)w1 + bj * 448 + gw * 128;
    unsigned w1v = (unsigned)((r >> 2) * (DFF * 2) + (gw == 3 ? 0 : h * 64) + (lane & 3) * 16);
    const char* w2b = (const char*)w2 + bj * 128 + (size_t)gw * 28 * 16384;
    const char* wob = (const char*)wo + bj * 128 + (size_t)gw * 8 * 16384;
    const LAS char* xa8[8];
#pragma unroll
    for (int i = 0; i < 8; ++i) { xa8[i] = lds + L_X + r * 1024 + (((2 * i + h) ^ (r & 15)) * 16); asm volatile("" : "+v"(xa8[i])); }
    const LAS char* wrd = ringp + h * 1024 + trq * 64 + trcb * 32 + trp * 8;
    const LAS char* wpa = ringp + (gw >> 1) * 512 + (4 * h + trq) * 64 + trcb * 32 + trp * 8;
    const LAS char* wpb = ringp + ((gw >> 1) ^ 1) * 512 + (4 * h + trq) * 64 + trcb * 32 + trp * 8;
    unsigned tvo = (lane < ((0x01103838 >> (8 * gw)) & 0xff) ? lane : 0) * 128, tz = 0;
    u32x4 em = {EMPTY, EMPTY, EMPTY, EMPTY};
    f32x16 c00 = {}, c01 = {}, c10 = {}, c11 = {};
    asm volatile("" : "+v"(tvo), "+v"(tz), "+v"(em), "+v"(w1v), "+v"(wrd), "+v"(wpa), "+v"(wpb), "+v"(c00), "+v"(c01), "+v"(c10), "+v"(c11));
    BARRIER();
    asm volatile("" :: "s"(cw));
#define WDMA(u) do { if ((u) < 68) wait_vm<THR>(); if ((u) < 32) { glds16(w1b + (size_t)(u) * 16 * DFF * 2, w1v, ring + ((u) & 7) * 2048); glds16(w1b + (size_t)(u) * 16 * DFF * 2 + 8 * DFF * 2, w1v, ring + ((u) & 7) * 2048 + 1024); } \
      else if ((u) < 40) { glds16(wob + ((u) - 32) * 16384, wv1k, ring + ((u) & 7) * 2048); glds16(wob + ((u) - 32) * 16384 + 8192, wv1k, ring + ((u) & 7) * 2048 + 1024); } \
      else if ((u) < 68) { glds16(w2b + ((u) - 40) * 16384, wv1k, ring + ((u) & 7) * 2048); glds16(w2b + ((u) - 40) * 16384 + 8192, wv1k, ring + ((u) & 7) * 2048 + 1024); } } while (0)
#pragma unroll
    for (int u = 0; u < 8; ++u) WDMA(u);
    __builtin_amdgcn_raw_buffer_store_b128(em, rs, lv, WS_AF + (bi * 2 * KS_B + KS_H + bj * 4 + gw) * 1024, SC1);
    __builtin_amdgcn_raw_buffer_store_b128(em, rs, lv, WS_AF + ((bi * 2 + 1) * KS_B + KS_H + bj * 4 + gw) * 1024, SC1);
    {
      wait_vm<14>();
      u32x4 wa = tr_frag(wrd, 256), wb = tr_frag(wrd + 512, 256), xa = lds128(xa8[0]), xc = lds128(xa8[0] + 32768);
      WAIT_LGKM0();
      WDMA(8);
#pragma unroll
      for (int s = 0; s < 32; ++s) {
        u32x4 na, nb, nxa, nxc;
        if (s == 12 && gw != 0) asm volatile("s_mov_b32 m0, %4\n\ts_nop 0\n\tglobal_load_lds_dword %0, %1\n\tglobal_load_lds_dword %2, %3"
                   :: "v"(tvo), "s"(touch0), "v"(tz), "s"(touch1), "s"(lds0 + L_RED + gw * 256) : "memory");
        if (s + 1 < 32) {
          wait_vm<14>();
          na = tr_frag(wrd + ((s + 1) & 7) * 2048, 256); nb = tr_frag(wrd + ((s + 1) & 7) * 2048 + 512, 256);
          nxa = lds128(xa8[(s + 1) & 7] + ((s + 1) >> 3) * 256); nxc = lds128(xa8[(s + 1) & 7] + ((s + 1) >> 3) * 256 + 32768);
        }
        SBAR();
        c00 = mfma32(wa, xa, c00); c01 = mfma32(wa, xc, c01);
        if (gw != 3) { c10 = mfma32(wb, xa, c10); c11 = mfma32(wb, xc, c11); }
        SBAR();
        if (s + 1 < 32) { WAIT_LGKM0(); WDMA(s + 9); wa = na; wb = nb; xa = nxa; xc = nxc; }
      }
    }
    __builtin_amdgcn_s_setprio(3);
    wait_vm<0>();
    {
      const int ho = WS_AF + (bi * 2 * KS_B + (bj * 7 + gw * 2) * 2) * 1024;
      __builtin_amdgcn_raw_buffer_store_b128(pack8relu(c00, 0), rs, lv, ho, SC1);
      __builtin_amdgcn_raw_buffer_store_b128(pack8relu(c00, 8), rs, lv + 1024, ho, SC1);
      __builtin_amdgcn_raw_buffer_store_b128(pack8relu(c01, 0), rs, lv, ho + KS_B * 1024, SC1);
      __builtin_amdgcn_raw_buffer_store_b128(pack8relu(c01, 8), rs, lv + 1024, ho + KS_B * 1024, SC1);
      if (gw != 3) {
        __builtin_amdgcn_raw_buffer_store_b128(pack8relu(c10, 0), rs, lv + 2048, ho, SC1);
        __builtin_amdgcn_raw_buffer_store_b128(pack8relu(c10, 8), rs, lv + 3072, ho, SC1);
        __builtin_amdgcn_raw_buffer_store_b128(pack8relu(c11, 0), rs, lv + 2048, ho + KS_B * 1024, SC1);
        __builtin_amdgcn_raw_buffer_store_b128(pack8relu(c11, 8), rs, lv + 3072, ho + KS_B * 1024, SC1);
      }
    }
    u32x32 wo0, wo1;
#pragma unroll
    for (int j = 0; j < 8; ++j) {
      const u32x4 fa = tr_frag(wpa + j * 2048, 1024), fb = tr_frag(wpb + j * 2048, 1024);
#pragma unroll
      for (int k = 0; k < 4; ++k) { if (j < 4) { wo0[8 * j + k] = fa[k]; wo0[8 * j + 4 + k] = fb[k]; } else { wo1[8 * (j - 4) + k] = fa[k]; wo1[8 * (j - 4) + 4 + k] = fb[k]; } }
    }
    asm volatile("" : "+{v[80:111]}"(wo0), "+{v[112:143]}"(wo1));
    WAIT_LGKM0();
    wait_vm<0>();
    if (lane == 0 && (__hip_atomic_fetch_add(cnt + 1, 1u, __ATOMIC_RELAXED, __HIP_MEMORY_SCOPE_WORKGROUP) & 15u) == 3u)
      __builtin_amdgcn_raw_buffer_store_b32(tag, rs, WS_FA + (bi * 8 + bj) * 128, 0, SC1);
#pragma unroll
    for (int u = 40; u < 48; ++u) WDMA(u);
    if (gw == 3) {
      f32x4 ga[4] = {};
      u32x4 gf[2], gx[2][4];
#pragma unroll
      for (int s = 0; s < 17; ++s) {
        if (s < 16) {
          gf[s & 1] = lds128(lds + L_GW + s * 64 + g4 * 16);
#pragma unroll
          for (int i = 0; i < 4; ++i) gx[s & 1][i] = lds128(lds + L_X + (i * 16 + l16) * 1024 + (s >> 2) * 256 + (((4 * (s & 3) + g4) ^ l16) * 16));
        }
        SBAR();
        if (s > 0) {
#pragma unroll
          for (int i = 0; i < 4; ++i) ga[i] = mfma16(gf[(s - 1) & 1], gx[(s - 1) & 1][i], ga[i]);
        }
        SBAR();
      }
      const float z = g4 == 0 ? ga[0][0] : g4 == 1 ? ga[1][0] : g4 == 2 ? ga[2][0] : ga[3][0];
      ((LAS float*)(lds + L_G))[lane] = 1.f / (1.f + __expf(-z));
      WAIT_LGKM0();
      if (lane == 0) __hip_atomic_fetch_add(cnt + 1, 16u, __ATOMIC_RELAXED, __HIP_MEMORY_SCOPE_WORKGROUP);
    }
    wait_two_flags(rs, WS_FA + (bi * 8 + 2 * gw) * 128, tag, lane);
    wait_vm<0>();
    c00 = f32x16{}; c01 = f32x16{}; c10 = f32x16{}; c11 = f32x16{};
    u32x4 ha[10], hb[10];
    const unsigned ra = WS_AF + (bi * 2 + (gw & 1)) * KS_B * 1024, rb = WS_AF + (bi * 2 + ((gw & 1) ^ 1)) * KS_B * 1024;
    {
      const unsigned hs = gw * 28 * 1024;
#pragma unroll
      for (int j = 0; j < 8; ++j) { wait_vm<THR>(); ld_sc1(ha[j], rsv, lv, ra + hs + j * 1024); ld_sc1(hb[j], rsv, lv, rb + hs + j * 1024); }
      const LAS char* wxa = wpa + (L_WKV - L_WQ - gw * 12288);
      const LAS char* wxb = wpb + (L_WKV - L_WQ - gw * 12288);
      asm volatile("" : "+v"(wxa), "+v"(wxb));
      u32x4 wa = tr_frag(wpa, 1024), wb = tr_frag(wpb, 1024);
      WAIT_LGKM0();
      WDMA(48);
      __builtin_amdgcn_s_setprio(0);
#pragma unroll
      for (int j = 0; j < 28; ++j) {
        if (j == HOLD_J - 6) {
          unsigned long long p2; unsigned dead, o1 = gw * 1024, o2 = gw * 768;
          asm volatile("s_getpc_b64 %0\n\t.Lgemm_pc:\n\ts_load_dword %1, %0, %2 offset:1024\n\ts_load_dword %1, %0, %2 offset:1152\n\ts_load_dword %1, %0, %2 offset:1280\n\ts_load_dword %1, %0, %2 offset:1408\n\t"
                       "s_load_dword %1, %0, %2 offset:1536\n\ts_load_dword %1, %0, %2 offset:1664\n\ts_load_dword %1, %0, %2 offset:1792\n\ts_load_dword %1, %0, %2 offset:1920\n\t"
                       "s_add_u32 %3, %3, .Lattn_last-.Lgemm_pc\n\t"
                       "s_load_dword %1, %0, %3 offset:0\n\ts_load_dword %1, %0, %3 offset:128\n\ts_load_dword %1, %0, %3 offset:256\n\ts_load_dword %1, %0, %3 offset:384\n\t"
                       "s_load_dword %1, %0, %3 offset:512\n\ts_load_dword %1, %0, %3 offset:640\n\ts_waitcnt lgkmcnt(0)"
                       : "=&s"(p2), "=&s"(dead), "+s"(o1), "+s"(o2) :: "memory", "scc");
        }
        if (j == HOLD_J) { __builtin_amdgcn_s_setprio(3); lds_wait<4>(cnt + 0, HOLD_N); __builtin_amdgcn_s_setprio(0); }
        const int k = j < 26 ? j & 7 : j - 18;
        asm volatile("s_waitcnt vmcnt(%2)" : "+v"(ha[k]), "+v"(hb[k]) : "n"(g2_younger(j)) : "memory");
        u32x4 na, nb;
        if (j + 1 < 26) { na = tr_frag(wpa + ((j + 1) & 7) * 2048, 1024); nb = tr_frag(wpb + ((j + 1) & 7) * 2048, 1024); }
        else if (j + 1 < 28) { na = tr_frag(wxa + (j - 25) * 2048, 1024); nb = tr_frag(wxb + (j - 25) * 2048, 1024); }
        SBAR();
        c00 = mfma32(wa, ha[k], c00); c01 = mfma32(wa, hb[k], c01); c10 = mfma32(wb, ha[k], c10); c11 = mfma32(wb, hb[k], c11);
        SBAR();
        if (j == 4 || j == 8) {
          const int x = j >> 3;
          wait_vm<THR>();
          glds16(w2b + (26 + x) * 16384, wv1k, lds0 + L_WKV + gw * 4096 + x * 2048); glds16(w2b + (26 + x) * 16384 + 8192, wv1k, lds0 + L_WKV + gw * 4096 + x * 2048 + 1024);
          wait_vm<THR>();
          ld_sc1(ha[8 + x], rsv, lv, ra + hs + (26 + x) * 1024); ld_sc1(hb[8 + x], rsv, lv, rb + hs + (26 + x) * 1024);
        }
        if (j + 1 < 28) { if (j + 9 < 26) { WAIT_LGKM0(); WDMA(40 + j + 9); } wa = na; wb = nb; }
        if (j + 8 < 26) { wait_vm<THR>(); ld_sc1(ha[j & 7], rsv, lv, ra + hs + (j + 8) * 1024); ld_sc1(hb[j & 7], rsv, lv, rb + hs + (j + 8) * 1024); }
      }
    }
    const float s0 = 1.f - ((const LAS float*)(lds + L_G))[(gw & 1) * 32 + r], s1 = 1.f - ((const LAS float*)(lds + L_G))[((gw & 1) ^ 1) * 32 + r];
#pragma unroll
    for (int i = 0; i < 16; ++i) { c00[i] *= s0; c10[i] *= s0; c01[i] *= s1; c11[i] *= s1; }
    asm volatile("" : "+{v[16:31]}"(c00), "+{v[32:47]}"(c01), "+{v[48:63]}"(c10), "+{v[64:79]}"(c11));
    lds_wait<1>(cnt + 2, 4u);
    __builtin_amdgcn_s_sleep(6);
    {
      const unsigned os = (KS_H + gw * 8) * 1024;
      unsigned cap = SPIN_CAP, tmp;
      asm volatile(GEMM_TAIL_ASM
                   : "+{v[16:31]}"(c00), "+{v[32:47]}"(c01), "+{v[48:63]}"(c10), "+{v[64:79]}"(c11), [cap] "+s"(cap), [t] "=&s"(tmp)
                   : "{v[80:111]}"(wo0), "{v[112:143]}"(wo1), [lv] "v"(lv), [rs] "s"(rsv), [a0] "s"(ra + os), [b0] "s"(rb + os), [a1] "s"(ra + os + 4096), [b1] "s"(rb + os + 4096),
                     [sp] "v"(lds0 + L_WQ + gw * 16384 + lv)
                   : "memory", "scc", "vcc", GT_CLOBBER);
    }
    {
      const LAS f32x4* r1 = (const LAS f32x4*)(lds + L_WQ + (gw ^ 1) * 16384 + lane * 16);
      const LAS f32x4* r2 = (const LAS f32x4*)(lds + L_WQ + (gw ^ 2) * 16384 + 4096 + lane * 16);
      const LAS f32x4* r3 = (const LAS f32x4*)(lds + L_WQ + (gw ^ 3) * 16384 + 8192 + lane * 16);
      unsigned pk[8];
#pragma unroll
      for (int q = 0; q < 4; ++q) {
        const f32x4 s = (f32x4{c00[4 * q], c00[4 * q + 1], c00[4 * q + 2], c00[4 * q + 3]} + r1[q * 64]) + (r2[q * 64] + r3[q * 64]);
        pk[2 * q] = pk2(s[0], s[1]); pk[2 * q + 1] = pk2(s[2], s[3]);
      }
      u16* op = out + (size_t)(bi * 64 + (gw & 1) * 32 + r) * D + bj * 64 + (gw >> 1) * 32 + h * 8;
#pragma unroll
      for (int p = 0; p < 2; ++p) {
        const auto s0v = __builtin_amdgcn_permlane32_swap(pk[4 * p], pk[4 * p + 2], false, false);
        const auto s1v = __builtin_amdgcn_permlane32_swap(pk[4 * p + 1], pk[4 * p + 3], false, false);
        *(u32x4*)(op + p * 16) = u32x4{s0v[0], s1v[0], s0v[1], s1v[1]};
      }
    }
  }
}

extern "C" void kernel_launch(void* const* d_in, const int* in_sizes, int n_in, void* d_out, int out_size, void* d_ws, size_t ws_size,
                              hipStream_t stream) {
  (void)hipFuncSetAttribute((const void*)fused_block, hipFuncAttributeMaxDynamicSharedMemorySize, L_BYTES);
  fused_block<<<256, 512, L_BYTES, stream>>>((const u16*)d_in[0], (const u16*)d_in[1], (const u16*)d_in[2], (const u16*)d_in[3], (const u16*)d_in[4],
                                             (const u16*)d_in[6], (const u16*)d_in[7], (const u16*)d_in[8], (char*)d_ws, (u16*)d_out);
}
```
